```python
import torch

def _softmax(x, axis=-1):
    return torch.softmax(x, dim=axis)
def _gelu(x):
    return torch.nn.functional.gelu(x, approximate="tanh")
def _silu(x):
    return torch.nn.functional.silu(x)
def _sigmoid(x):
    return torch.sigmoid(x)
def _layernorm(x, axis=-1, eps=1e-5):
    m = x.mean(dim=axis, keepdim=True)
    v = x.var(dim=axis, keepdim=True, unbiased=False)
    return (x - m) / torch.sqrt(v + eps)
def _scatter_add(tgt, idx, src, axis=0):
    return tgt.clone().index_add_(axis, idx, src)
def _segment_sum(src, seg, n):
    out = torch.zeros((n,) + src.shape[1:], dtype=src.dtype)
    return out.index_add_(0, seg, src)
def _pad(x, pw):
    flat = tuple(v for pair in reversed(pw) for v in pair)
    return torch.nn.functional.pad(x, flat)

kernel_name = 'standalone__gqa_softmax__sampling'

def setup_inputs():

    _w = (lambda a: (lambda t: t.to(torch.bfloat16) if t.is_floating_point() else t)(torch.as_tensor(a)))
    B, Hq, Hkv, Sq, Sk, D = 16, 32, 8, 1, 4096, 128
    return {
        "q": _w((lambda *s: torch.randn(*s, dtype=torch.bfloat16) * 0.8)(B, Hq, Sq, D)),
        "k": _w((lambda *s: torch.randn(*s, dtype=torch.bfloat16) * 0.8)(B, Hkv, Sk, D)),
        "v": _w((lambda *s: torch.randn(*s, dtype=torch.bfloat16) * 0.05)(B, Hkv, Sk, D)),
    }

def reference(q, k, v):
    B, Hq, Sq, D = q.shape
    Hkv = k.shape[1]
    rep = Hq // Hkv
    k = (lambda x, n, axis: torch.repeat_interleave(x, n, dim=axis))(k, rep, axis=1)
    v = (lambda x, n, axis: torch.repeat_interleave(x, n, dim=axis))(v, rep, axis=1)
    scale = D ** -0.5
    scores = torch.einsum("bhqd,bhkd->bhqk", q, k) * scale
    p = _softmax(scores, axis=-1)
    return torch.einsum("bhqk,bhkd->bhqd", p, v)
```

```cpp
#include <hip/hip_runtime.h>
#include <hip/hip_bf16.h>
#include <stdint.h>

namespace gqa {

constexpr int B = 16, HQ = 32, HKV = 8, SK = 4096, D = 128, G = HQ / HKV;
constexpr int NSPLIT = 2;
constexpr int NWAVE = 8;
constexpr int NTHR = NWAVE * 64;
constexpr int KEYS_WG = SK / NSPLIT;
constexpr int KEYS_W = KEYS_WG / NWAVE;
constexpr int NL = KEYS_W / 4;
constexpr int WIN = 32;
constexpr float SCALE_LOG2E = 0.08838834764831845f * 1.4426950408889634f;

typedef unsigned u32x4 __attribute__((ext_vector_type(4)));
typedef float f32x4 __attribute__((ext_vector_type(4)));
typedef __bf16 bf16x8 __attribute__((ext_vector_type(8)));
typedef __bf16 bf16x2 __attribute__((ext_vector_type(2)));
#define GAS __attribute__((address_space(1)))

__device__ __forceinline__ u32x4 ldnt(const GAS u32x4* p) { return __builtin_nontemporal_load(p); }
__device__ __forceinline__ f32x4 mfma16(u32x4 a, u32x4 b, f32x4 c) {
  return __builtin_amdgcn_mfma_f32_16x16x32_bf16(__builtin_bit_cast(bf16x8, a), __builtin_bit_cast(bf16x8, b), c, 0, 0, 0);
}
template <int CTRL> __device__ __forceinline__ float dpp(float x) {
  return __builtin_bit_cast(float, __builtin_amdgcn_mov_dpp(__builtin_bit_cast(int, x), CTRL, 0xf, 0xf, true));
}
__device__ __forceinline__ float xrow_max(float x) {
  auto s = __builtin_amdgcn_permlane16_swap(__float_as_uint(x), __float_as_uint(x), false, false);
  x = fmaxf(__uint_as_float(s[0]), __uint_as_float(s[1]));
  auto t = __builtin_amdgcn_permlane32_swap(__float_as_uint(x), __float_as_uint(x), false, false);
  return fmaxf(__uint_as_float(t[0]), __uint_as_float(t[1]));
}
__device__ __forceinline__ float xrow_sum(float x) {
  auto s = __builtin_amdgcn_permlane16_swap(__float_as_uint(x), __float_as_uint(x), false, false);
  x = __uint_as_float(s[0]) + __uint_as_float(s[1]);
  auto t = __builtin_amdgcn_permlane32_swap(__float_as_uint(x), __float_as_uint(x), false, false);
  return __uint_as_float(t[0]) + __uint_as_float(t[1]);
}
__device__ __forceinline__ unsigned pack_bf16(float lo, float hi) {
  bf16x2 r = {(__bf16)lo, (__bf16)hi};
  return __builtin_bit_cast(unsigned, r);
}

constexpr size_t WS_O = 0;
constexpr size_t WS_ML = (size_t)B * HKV * NSPLIT * G * D * 4;

constexpr int LDS_REC = 4 * D + 8;

__global__ void __launch_bounds__(NTHR, 2)
attn_split(const unsigned short* __restrict__ q, const unsigned short* __restrict__ k, const unsigned short* __restrict__ v,
           float* __restrict__ ws) {
  __shared__ __attribute__((aligned(16))) float lds[NWAVE * LDS_REC];
  const int tid = threadIdx.x, lane = tid & 63;
  const int w = __builtin_amdgcn_readfirstlane(tid >> 6);
  const int bh = blockIdx.x >> 1, split = blockIdx.x & 1;
  const size_t key0 = (size_t)bh * SK + split * KEYS_WG + w * KEYS_W;
  const int koff = ((lane >> 2) & 3) * 16 + (lane >> 4) * 4 + (lane & 3);
  const GAS u32x4* kp = (const GAS u32x4*)(k + key0 * D) + koff;
  const GAS u32x4* vp = (const GAS u32x4*)(v + key0 * D) + lane;
  const u32x4 qf = *((const GAS u32x4*)(q + (size_t)bh * G * D) + koff);

  u32x4 ring[WIN];
#pragma unroll
  for (int i = 0; i < WIN; ++i) ring[i] = ldnt(kp + i * 64);
  __builtin_amdgcn_sched_barrier(0);

  float s[NL];
#pragma unroll
  for (int i = 0; i < NL; ++i) {
    const u32x4 cur = ring[i % WIN];
    const int n = i + WIN;
    ring[i % WIN] = (n < NL) ? ldnt(kp + n * 64) : ldnt(vp + (n - NL) * 64);
    const f32x4 d = mfma16(cur, qf, f32x4{0.f, 0.f, 0.f, 0.f});
    s[i] = (dpp<0x00>(d[0]) + dpp<0x55>(d[1])) + (dpp<0xAA>(d[2]) + dpp<0xFF>(d[3]));
    __builtin_amdgcn_sched_barrier(0);
  }
  float m = s[0];
#pragma unroll
  for (int i = 1; i < NL; ++i) m = fmaxf(m, s[i]);
  m = xrow_max(m);
  const float mc = m * SCALE_LOG2E;
  float l = 0.f;
  unsigned pk[NL / 2];
#pragma unroll
  for (int i = 0; i < NL; i += 2) {
    const float p0 = __builtin_amdgcn_exp2f(fmaf(s[i], SCALE_LOG2E, -mc));
    const float p1 = __builtin_amdgcn_exp2f(fmaf(s[i + 1], SCALE_LOG2E, -mc));
    l += p0 + p1;
    pk[i / 2] = pack_bf16(p0, p1);
  }
  l = xrow_sum(l);
  __builtin_amdgcn_sched_barrier(0);

  f32x4 acc[8];
#pragma unroll
  for (int e = 0; e < 8; ++e) acc[e] = f32x4{0.f, 0.f, 0.f, 0.f};
#pragma unroll
  for (int I = 0; I < NL / 8; ++I) {
    u32x4 vv[8];
#pragma unroll
    for (int j = 0; j < 8; ++j) {
      const int i = NL + I * 8 + j;
      vv[j] = ring[i % WIN];
      if (i + WIN < 2 * NL) ring[i % WIN] = ldnt(vp + (i + WIN - NL) * 64);
    }
    const u32x4 pa = {pk[4 * I], pk[4 * I + 1], pk[4 * I + 2], pk[4 * I + 3]};
#pragma unroll
    for (int wd = 0; wd < 4; ++wd) {
      u32x4 blo, bhi;
#pragma unroll
      for (int mm = 0; mm < 4; ++mm) {
        blo[mm] = __builtin_amdgcn_perm(vv[2 * mm + 1][wd], vv[2 * mm][wd], 0x05040100u);
        bhi[mm] = __builtin_amdgcn_perm(vv[2 * mm + 1][wd], vv[2 * mm][wd], 0x07060302u);
      }
      acc[2 * wd] = mfma16(pa, blo, acc[2 * wd]);
      acc[2 * wd + 1] = mfma16(pa, bhi, acc[2 * wd + 1]);
    }
    __builtin_amdgcn_sched_barrier(0);
  }
  float* rec = lds + w * LDS_REC;
  {
    f32x4 o0 = {acc[0][0], acc[1][0], acc[2][0], acc[3][0]}, o1 = {acc[4][0], acc[5][0], acc[6][0], acc[7][0]};
    *(f32x4*)(rec + lane * 8) = o0;
    *(f32x4*)(rec + lane * 8 + 4) = o1;
    if ((lane & ~12) == 0) { rec[4 * D + (lane >> 2)] = mc; rec[4 * D + 4 + (lane >> 2)] = l; }
  }
  __syncthreads();
  {
    const int h = tid >> 7;
    float M = -INFINITY;
#pragma unroll
    for (int ww = 0; ww < NWAVE; ++ww) M = fmaxf(M, lds[ww * LDS_REC + 4 * D + h]);
    float L = 0.f, o = 0.f;
#pragma unroll
    for (int ww = 0; ww < NWAVE; ++ww) {
      const float f = __builtin_amdgcn_exp2f(lds[ww * LDS_REC + 4 * D + h] - M);
      L = fmaf(f, lds[ww * LDS_REC + 4 * D + 4 + h], L);
      o = fmaf(f, lds[ww * LDS_REC + tid], o);
    }
    ws[(size_t)blockIdx.x * (G * D) + tid] = o;
    if ((tid & 127) == 0) {
      float* ml = ws + WS_ML / 4 + (size_t)blockIdx.x * (2 * G) + 2 * h;
      ml[0] = M; ml[1] = L;
    }
  }
}

__global__ void __launch_bounds__(512) attn_combine(const float* __restrict__ ws, __hip_bfloat16* __restrict__ out) {
  const int bh = blockIdx.x, tid = threadIdx.x, h = tid >> 7;
  const float* ml = ws + WS_ML / 4 + (size_t)bh * NSPLIT * (2 * G);
  float M = -INFINITY;
#pragma unroll
  for (int s = 0; s < NSPLIT; ++s) M = fmaxf(M, ml[s * 2 * G + 2 * h]);
  float L = 0.f, o = 0.f;
#pragma unroll
  for (int s = 0; s < NSPLIT; ++s) {
    const float f = __builtin_amdgcn_exp2f(ml[s * 2 * G + 2 * h] - M);
    L = fmaf(f, ml[s * 2 * G + 2 * h + 1], L);
    o = fmaf(f, ws[((size_t)bh * NSPLIT + s) * (G * D) + tid], o);
  }
  out[(size_t)bh * (G * D) + tid] = __float2bfloat16(o / L);
}

}

extern "C" void kernel_launch(void* const* d_in, const int* in_sizes, int n_in, void* d_out, int out_size, void* d_ws,
                              size_t ws_size, hipStream_t stream) {
  using namespace gqa;
  attn_split<<<B * HKV * NSPLIT, NTHR, 0, stream>>>((const unsigned short*)d_in[0], (const unsigned short*)d_in[1],
                                                    (const unsigned short*)d_in[2], (float*)d_ws);
  attn_combine<<<B * HKV, 512, 0, stream>>>((const float*)d_ws, (__hip_bfloat16*)d_out);
}
```

```cpp
#include <hip/hip_runtime.h>
#include <hip/hip_bf16.h>
#include <stdint.h>

namespace gqa {

constexpr int B = 16, HQ = 32, HKV = 8, SK = 4096, D = 128, G = HQ / HKV;
constexpr int NSPLIT = 2;
constexpr int NWAVE = 4;
constexpr int NTHR = NWAVE * 64;
constexpr int KEYS_WG = SK / NSPLIT;
constexpr int NL = KEYS_WG / NWAVE / 4;
#define WIN 16
constexpr int NIT = NL / WIN;
constexpr unsigned WG_BYTES = KEYS_WG * D * 2;
constexpr float SCALE_LOG2E = 0.08838834764831845f * 1.4426950408889634f;

typedef unsigned u32x4 __attribute__((ext_vector_type(4)));
typedef float f32x4 __attribute__((ext_vector_type(4)));
typedef __bf16 bf16x8 __attribute__((ext_vector_type(8)));
typedef __bf16 bf16x2 __attribute__((ext_vector_type(2)));
#define GAS __attribute__((address_space(1)))

__device__ __forceinline__ u32x4 ldnt(__amdgpu_buffer_rsrc_t r, int voff, int soff) {
  return __builtin_amdgcn_raw_buffer_load_b128(r, voff, soff, 2  );
}
__device__ __forceinline__ f32x4 mfma16(u32x4 a, u32x4 b, f32x4 c) {
  return __builtin_amdgcn_mfma_f32_16x16x32_bf16(__builtin_bit_cast(bf16x8, a), __builtin_bit_cast(bf16x8, b), c, 0, 0, 0);
}
template <int CTRL> __device__ __forceinline__ float dpp(float x) {
  return __builtin_bit_cast(float, __builtin_amdgcn_mov_dpp(__builtin_bit_cast(int, x), CTRL, 0xf, 0xf, true));
}
__device__ __forceinline__ float xrow_max(float x) {
  auto s = __builtin_amdgcn_permlane16_swap(__float_as_uint(x), __float_as_uint(x), false, false);
  x = fmaxf(__uint_as_float(s[0]), __uint_as_float(s[1]));
  auto t = __builtin_amdgcn_permlane32_swap(__float_as_uint(x), __float_as_uint(x), false, false);
  return fmaxf(__uint_as_float(t[0]), __uint_as_float(t[1]));
}
__device__ __forceinline__ float xrow_sum(float x) {
  auto s = __builtin_amdgcn_permlane16_swap(__float_as_uint(x), __float_as_uint(x), false, false);
  x = __uint_as_float(s[0]) + __uint_as_float(s[1]);
  auto t = __builtin_amdgcn_permlane32_swap(__float_as_uint(x), __float_as_uint(x), false, false);
  return __uint_as_float(t[0]) + __uint_as_float(t[1]);
}
__device__ __forceinline__ unsigned pack_bf16(float lo, float hi) {
  bf16x2 r = {(__bf16)lo, (__bf16)hi};
  return __builtin_bit_cast(unsigned, r);
}
__device__ __forceinline__ int stream_off(int i, int w) { return ((i >> 2) * (4 * NWAVE) + 4 * w + (i & 3)) * 1024; }

constexpr size_t WS_O = 0;
constexpr size_t WS_ML = (size_t)B * HKV * NSPLIT * G * D * 4;

constexpr int LDS_SC = NWAVE * (NL / 4) * 64;
constexpr int LDS_REC = 4 * D + 8;

__global__ void __launch_bounds__(NTHR, 2)
attn_split(const unsigned short* __restrict__ q, const unsigned short* __restrict__ k, const unsigned short* __restrict__ v,
           float* __restrict__ ws) {
  __shared__ __attribute__((aligned(16))) float lds[LDS_SC + NWAVE * LDS_REC];
  const int tid = threadIdx.x, lane = tid & 63;
  const int w = __builtin_amdgcn_readfirstlane(tid >> 6);
  const int bh = blockIdx.x >> 1;
  const __amdgpu_buffer_rsrc_t rk = __builtin_amdgcn_make_buffer_rsrc((void*)(k + (size_t)blockIdx.x * (KEYS_WG * D)), 0, WG_BYTES, 0x00020000);
  const __amdgpu_buffer_rsrc_t rv = __builtin_amdgcn_make_buffer_rsrc((void*)(v + (size_t)blockIdx.x * (KEYS_WG * D)), 0, WG_BYTES, 0x00020000);
  const int koff = ((lane >> 2) & 3) * 256 + (lane >> 4) * 64 + (lane & 3) * 16;
  const int voff = lane * 16;
  const int p = lane & 3;
  const u32x4 qf = *(const GAS u32x4*)((const GAS char*)q + (size_t)bh * (G * D * 2) + koff);

  u32x4 ring[WIN];
#pragma unroll
  for (int i = 0; i < WIN; ++i) { ring[i] = ldnt(rk, koff, stream_off(i, w)); __builtin_amdgcn_sched_barrier(0); }

  f32x4* sc = (f32x4*)lds + w * (NL / 16) * 64 + lane;
  float m = -INFINITY;
  for (int it = 0; it < NIT; ++it) {
    const bool last = it == NIT - 1;
    const __amdgpu_buffer_rsrc_t rn = last ? rv : rk;
    const int noff = last ? voff : koff;
    const int nbase = last ? stream_off(0, w) : stream_off((it + 1) * WIN, w);
    f32x4 sp[WIN / 16];
#pragma unroll
    for (int n = 0; n < WIN / 4; ++n) {
      f32x4 d[4];
#pragma unroll
      for (int j = 0; j < 4; ++j) {
        d[j] = mfma16(ring[4 * n + j], qf, f32x4{0.f, 0.f, 0.f, 0.f});
        ring[4 * n + j] = ldnt(rn, noff, nbase + stream_off(4 * n + j, 0));
        __builtin_amdgcn_sched_barrier(0);
      }
      float sv = 0.f;
#pragma unroll
      for (int j = 0; j < 4; ++j) {
        float s = dpp<0x00>(d[j][0]);
        s += dpp<0x55>(d[j][1]);
        s += dpp<0xAA>(d[j][2]);
        s += dpp<0xFF>(d[j][3]);
        asm("" : "+v"(s));
        sv = (j == 0) ? s : ((p == j) ? s : sv);
      }
      m = fmaxf(m, sv);
      sp[n >> 2][n & 3] = sv;
      __builtin_amdgcn_sched_barrier(0);
    }
#pragma unroll
    for (int u = 0; u < WIN / 16; ++u) sc[(it * (WIN / 16) + u) * 64] = sp[u];
  }
  m = fmaxf(m, dpp<0xB1>(m));
  m = fmaxf(m, dpp<0x4E>(m));
  m = xrow_max(m);
  const float mc = m * SCALE_LOG2E;
  const unsigned lo_sel = (p & 1) ? 0x01000c0cu : 0x0c0c0100u, hi_sel = (p & 1) ? 0x03020c0cu : 0x0c0c0302u, zsel = 0x0c0c0c0cu;
  const unsigned sel0 = p < 2 ? lo_sel : zsel, sel1 = p < 2 ? zsel : lo_sel, sel2 = p < 2 ? hi_sel : zsel, sel3 = p < 2 ? zsel : hi_sel;
  float l = 0.f;
  f32x4 acc[8];
#pragma unroll
  for (int e = 0; e < 8; ++e) acc[e] = f32x4{0.f, 0.f, 0.f, 0.f};
  for (int it = 0; it < NIT; ++it) {
    const int nbase = stream_off((it + 1) * WIN, w);
    f32x4 sp[WIN / 16];
#pragma unroll
    for (int u = 0; u < WIN / 16; ++u) sp[u] = sc[(it * (WIN / 16) + u) * 64];
#pragma unroll
    for (int g = 0; g < WIN / 8; ++g) {
      const float p0 = __builtin_amdgcn_exp2f(fmaf(sp[g >> 1][(2 * g) & 3], SCALE_LOG2E, -mc));
      const float p1 = __builtin_amdgcn_exp2f(fmaf(sp[g >> 1][(2 * g + 1) & 3], SCALE_LOG2E, -mc));
      l += p0 + p1;
      const unsigned pk = pack_bf16(p0, p1);
      const u32x4 pa = {__builtin_amdgcn_perm(0u, pk, sel0), __builtin_amdgcn_perm(0u, pk, sel1), __builtin_amdgcn_perm(0u, pk, sel2),
                        __builtin_amdgcn_perm(0u, pk, sel3)};
      u32x4 blo[4], bhi[4];
#pragma unroll
      for (int mm = 0; mm < 4; ++mm) {
        const u32x4 a = ring[8 * g + 2 * mm], b = ring[8 * g + 2 * mm + 1];
#pragma unroll
        for (int wd = 0; wd < 4; ++wd) {
          blo[wd][mm] = __builtin_amdgcn_perm(b[wd], a[wd], 0x05040100u);
          bhi[wd][mm] = __builtin_amdgcn_perm(b[wd], a[wd], 0x07060302u);
        }
        __builtin_amdgcn_sched_barrier(0);
        ring[8 * g + 2 * mm] = ldnt(rv, voff, nbase + stream_off(8 * g + 2 * mm, 0));
        ring[8 * g + 2 * mm + 1] = ldnt(rv, voff, nbase + stream_off(8 * g + 2 * mm + 1, 0));
        __builtin_amdgcn_sched_barrier(0);
      }
#pragma unroll
      for (int wd = 0; wd < 4; ++wd) {
        acc[2 * wd] = mfma16(pa, blo[wd], acc[2 * wd]);
        acc[2 * wd + 1] = mfma16(pa, bhi[wd], acc[2 * wd + 1]);
      }
      __builtin_amdgcn_sched_barrier(0);
    }
  }
  l += dpp<0xB1>(l);
  l += dpp<0x4E>(l);
  l = xrow_sum(l);
  float* rec = lds + LDS_SC + w * LDS_REC;
  {
    f32x4 o0, o1;
#pragma unroll
    for (int e = 0; e < 4; ++e) {
      o0[e] = (acc[e][0] + acc[e][1]) + (acc[e][2] + acc[e][3]);
      o1[e] = (acc[4 + e][0] + acc[4 + e][1]) + (acc[4 + e][2] + acc[4 + e][3]);
    }
    *(f32x4*)(rec + lane * 8) = o0;
    *(f32x4*)(rec + lane * 8 + 4) = o1;
    if ((lane & ~12) == 0) { rec[4 * D + (lane >> 2)] = mc; rec[4 * D + 4 + (lane >> 2)] = l; }
  }
  __syncthreads();
#pragma unroll
  for (int half = 0; half < 2; ++half) {
    const int o_idx = tid + half * 256;
    const int h = o_idx >> 7;
    const float* r0 = lds + LDS_SC;
    float M = -INFINITY;
#pragma unroll
    for (int ww = 0; ww < NWAVE; ++ww) M = fmaxf(M, r0[ww * LDS_REC + 4 * D + h]);
    float L = 0.f, o = 0.f;
#pragma unroll
    for (int ww = 0; ww < NWAVE; ++ww) {
      const float f = __builtin_amdgcn_exp2f(r0[ww * LDS_REC + 4 * D + h] - M);
      L = fmaf(f, r0[ww * LDS_REC + 4 * D + 4 + h], L);
      o = fmaf(f, r0[ww * LDS_REC + o_idx], o);
    }
    ws[(size_t)blockIdx.x * (G * D) + o_idx] = o;
    if ((o_idx & 127) == 0) {
      float* ml = ws + WS_ML / 4 + (size_t)blockIdx.x * (2 * G) + 2 * h;
      ml[0] = M; ml[1] = L;
    }
  }
}

__global__ void __launch_bounds__(512) attn_combine(const float* __restrict__ ws, __hip_bfloat16* __restrict__ out) {
  const int bh = blockIdx.x, tid = threadIdx.x, h = tid >> 7;
  const float* ml = ws + WS_ML / 4 + (size_t)bh * NSPLIT * (2 * G);
  float M = -INFINITY;
#pragma unroll
  for (int s = 0; s < NSPLIT; ++s) M = fmaxf(M, ml[s * 2 * G + 2 * h]);
  float L = 0.f, o = 0.f;
#pragma unroll
  for (int s = 0; s < NSPLIT; ++s) {
    const float f = __builtin_amdgcn_exp2f(ml[s * 2 * G + 2 * h] - M);
    L = fmaf(f, ml[s * 2 * G + 2 * h + 1], L);
    o = fmaf(f, ws[((size_t)bh * NSPLIT + s) * (G * D) + tid], o);
  }
  out[(size_t)bh * (G * D) + tid] = __float2bfloat16(o / L);
}

}

extern "C" void kernel_launch(void* const* d_in, const int* in_sizes, int n_in, void* d_out, int out_size, void* d_ws,
                              size_t ws_size, hipStream_t stream) {
  using namespace gqa;
  attn_split<<<B * HKV * NSPLIT, NTHR, 0, stream>>>((const unsigned short*)d_in[0], (const unsigned short*)d_in[1],
                                                    (const unsigned short*)d_in[2], (float*)d_ws);
  attn_combine<<<B * HKV, 512, 0, stream>>>((const float*)d_ws, (__hip_bfloat16*)d_out);
}
```

```cpp
#include <hip/hip_runtime.h>
#include <hip/hip_bf16.h>
#include <stdint.h>

namespace gqa {
#define STAMP(i)
#define STAMP_FLUSH()

constexpr int B = 16, HQ = 32, HKV = 8, SK = 4096, D = 128, G = HQ / HKV;
constexpr int NSPLIT = 2;
constexpr int NWAVE = 4;
constexpr int NTHR = (NWAVE + 1) * 64;
constexpr int KEYS_WG = SK / NSPLIT;
constexpr int NL = KEYS_WG / NWAVE / 4;
#define WIN 16
constexpr int NIT = NL / WIN;
constexpr unsigned WG_BYTES = KEYS_WG * D * 2;
constexpr float SCALE_LOG2E = 0.08838834764831845f * 1.4426950408889634f;

typedef unsigned u32x4 __attribute__((ext_vector_type(4)));
typedef float f32x4 __attribute__((ext_vector_type(4)));
typedef __bf16 bf16x8 __attribute__((ext_vector_type(8)));
typedef __bf16 bf16x2 __attribute__((ext_vector_type(2)));
#define GAS __attribute__((address_space(1)))

__device__ __forceinline__ u32x4 ldnt(__amdgpu_buffer_rsrc_t r, int voff, int soff) {
  return __builtin_amdgcn_raw_buffer_load_b128(r, voff, soff, 2  );
}
__device__ __forceinline__ f32x4 mfma16(u32x4 a, u32x4 b, f32x4 c) {
  return __builtin_amdgcn_mfma_f32_16x16x32_bf16(__builtin_bit_cast(bf16x8, a), __builtin_bit_cast(bf16x8, b), c, 0, 0, 0);
}
template <int CTRL> __device__ __forceinline__ float dpp(float x) {
  return __builtin_bit_cast(float, __builtin_amdgcn_mov_dpp(__builtin_bit_cast(int, x), CTRL, 0xf, 0xf, true));
}
__device__ __forceinline__ float xrow_max(float x) {
  auto s = __builtin_amdgcn_permlane16_swap(__float_as_uint(x), __float_as_uint(x), false, false);
  x = fmaxf(__uint_as_float(s[0]), __uint_as_float(s[1]));
  auto t = __builtin_amdgcn_permlane32_swap(__float_as_uint(x), __float_as_uint(x), false, false);
  return fmaxf(__uint_as_float(t[0]), __uint_as_float(t[1]));
}
__device__ __forceinline__ float xrow_sum(float x) {
  auto s = __builtin_amdgcn_permlane16_swap(__float_as_uint(x), __float_as_uint(x), false, false);
  x = __uint_as_float(s[0]) + __uint_as_float(s[1]);
  auto t = __builtin_amdgcn_permlane32_swap(__float_as_uint(x), __float_as_uint(x), false, false);
  return __uint_as_float(t[0]) + __uint_as_float(t[1]);
}
__device__ __forceinline__ unsigned pack_bf16(float lo, float hi) {
  bf16x2 r = {(__bf16)lo, (__bf16)hi};
  return __builtin_bit_cast(unsigned, r);
}
__device__ __forceinline__ int stream_off(int i, int w) { return ((i >> 2) * (4 * NWAVE) + 4 * w + (i & 3)) * 1024; }

constexpr int WS_WG = 4096, WS_NONCE = 3072, WS_ECHO = 3584;
constexpr unsigned TAG = 0x6b1d39a5u;
constexpr unsigned SPIN_CAP = 1u << 20;
typedef GAS unsigned long long gu64;
#define RLX_AGENT __ATOMIC_RELAXED, __HIP_MEMORY_SCOPE_AGENT
__device__ __forceinline__ unsigned long long granule(float x) { return ((unsigned long long)TAG << 32) | __float_as_uint(x); }

constexpr int LDS_SC = NWAVE * (NL / 4) * 64;
constexpr int LDS_REC = 4 * D + 8;

__global__ void __launch_bounds__(NTHR, 2)
attn_split(const unsigned short* __restrict__ q, const unsigned short* __restrict__ k, const unsigned short* __restrict__ v,
           char* __restrict__ ws, unsigned short* __restrict__ out) {
  __shared__ __attribute__((aligned(16))) float lds[LDS_SC + NWAVE * LDS_REC];
  STAMP(0);
  asm volatile("" :: "s"(q), "s"(k), "s"(v), "s"(ws), "s"(out));
  const int tid = threadIdx.x, lane = tid & 63;
  const int w = __builtin_amdgcn_readfirstlane(tid >> 6);
  const int bh = blockIdx.x >> 1;
  GAS char* const ws_me = (GAS char*)ws + (size_t)blockIdx.x * WS_WG;
  GAS char* const ws_pt = (GAS char*)ws + (size_t)(blockIdx.x ^ 1) * WS_WG;
  if (w == NWAVE) {
    gu64* const my_nonce = (gu64*)(ws_me + WS_NONCE);
    gu64* const my_echo = (gu64*)(ws_me + WS_ECHO);
    gu64* const pt_nonce = (gu64*)(ws_pt + WS_NONCE);
    gu64* const pt_echo = (gu64*)(ws_pt + WS_ECHO);
    const unsigned long long nonce = __hip_atomic_load(my_nonce, RLX_AGENT) + 1ull;
    gu64* const inbox = (gu64*)ws_me;
#pragma unroll
    for (int i = 0; i < 5; ++i) if (i * 64 + lane < 260) __hip_atomic_store(inbox + i * 64 + lane, 0ull, RLX_AGENT);
    if (lane == 0) __hip_atomic_store(my_nonce, nonce, RLX_AGENT);
    asm volatile("s_waitcnt vmcnt(0)" ::: "memory");
#pragma nounroll
    for (unsigned spins = 0; spins < SPIN_CAP; ++spins) {
      const unsigned long long e = __hip_atomic_load(pt_nonce, RLX_AGENT);
      const unsigned long long r = __hip_atomic_load(pt_echo, RLX_AGENT);
      if (lane == 0) __hip_atomic_store(my_echo, e, RLX_AGENT);
      if (__builtin_amdgcn_readfirstlane((int)(r == nonce))) break;
      __builtin_amdgcn_s_sleep(16);
    }
    const unsigned long long e2 = __hip_atomic_load(pt_nonce, RLX_AGENT);
    if (lane == 0) __hip_atomic_store(my_echo, e2, RLX_AGENT);
    asm volatile("s_waitcnt vmcnt(0)" ::: "memory");
    __syncthreads();
    return;
  }
  const __amdgpu_buffer_rsrc_t rk = __builtin_amdgcn_make_buffer_rsrc((void*)(k + (size_t)blockIdx.x * (KEYS_WG * D)), 0, WG_BYTES, 0x00020000);
  const __amdgpu_buffer_rsrc_t rv = __builtin_amdgcn_make_buffer_rsrc((void*)(v + (size_t)blockIdx.x * (KEYS_WG * D)), 0, WG_BYTES, 0x00020000);
  const int koff = ((lane >> 2) & 3) * 256 + (lane >> 4) * 64 + (lane & 3) * 16;
  const int voff = lane * 16;
  const int p = lane & 3;
  const u32x4 qf = *(const GAS u32x4*)((const GAS char*)q + (size_t)bh * (G * D * 2) + koff);

  u32x4 ring[WIN];
#pragma unroll
  for (int i = 0; i < WIN; ++i) { ring[i] = ldnt(rk, koff, stream_off(i, w)); __builtin_amdgcn_sched_barrier(0); }

  STAMP(1);
  f32x4* sc = (f32x4*)lds + w * (NL / 16) * 64 + lane;
  float m = -INFINITY;
  for (int it = 0; it < NIT; ++it) {
    const bool last = it == NIT - 1;
    const __amdgpu_buffer_rsrc_t rn = last ? rv : rk;
    const int noff = last ? voff : koff;
    const int nbase = last ? stream_off(0, w) : stream_off((it + 1) * WIN, w);
    f32x4 sp[WIN / 16];
#pragma unroll
    for (int n = 0; n < WIN / 4; ++n) {
      f32x4 d[4];
#pragma unroll
      for (int j = 0; j < 4; ++j) {
        d[j] = mfma16(ring[4 * n + j], qf, f32x4{0.f, 0.f, 0.f, 0.f});
        ring[4 * n + j] = ldnt(rn, noff, nbase + stream_off(4 * n + j, 0));
        __builtin_amdgcn_sched_barrier(0);
      }
      float sv = 0.f;
#pragma unroll
      for (int j = 0; j < 4; ++j) {
        float s = dpp<0x00>(d[j][0]);
        s += dpp<0x55>(d[j][1]);
        s += dpp<0xAA>(d[j][2]);
        s += dpp<0xFF>(d[j][3]);
        asm("" : "+v"(s));
        sv = (j == 0) ? s : ((p == j) ? s : sv);
      }
      m = fmaxf(m, sv);
      sp[n >> 2][n & 3] = sv;
      __builtin_amdgcn_sched_barrier(0);
    }
#pragma unroll
    for (int u = 0; u < WIN / 16; ++u) sc[(it * (WIN / 16) + u) * 64] = sp[u];
  }
  STAMP(3);
  m = fmaxf(m, dpp<0xB1>(m));
  m = fmaxf(m, dpp<0x4E>(m));
  m = xrow_max(m);
  const float mc = m * SCALE_LOG2E;
  const unsigned lo_sel = (p & 1) ? 0x01000c0cu : 0x0c0c0100u, hi_sel = (p & 1) ? 0x03020c0cu : 0x0c0c0302u, zsel = 0x0c0c0c0cu;
  const unsigned sel0 = p < 2 ? lo_sel : zsel, sel1 = p < 2 ? zsel : lo_sel, sel2 = p < 2 ? hi_sel : zsel, sel3 = p < 2 ? zsel : hi_sel;
  float l = 0.f;
  f32x4 acc[8];
#pragma unroll
  for (int e = 0; e < 8; ++e) acc[e] = f32x4{0.f, 0.f, 0.f, 0.f};
  for (int it = 0; it < NIT; ++it) {
    const int nbase = stream_off((it + 1) * WIN, w);
    f32x4 sp[WIN / 16];
#pragma unroll
    for (int u = 0; u < WIN / 16; ++u) sp[u] = sc[(it * (WIN / 16) + u) * 64];
#pragma unroll
    for (int g = 0; g < WIN / 8; ++g) {
      const float p0 = __builtin_amdgcn_exp2f(fmaf(sp[g >> 1][(2 * g) & 3], SCALE_LOG2E, -mc));
      const float p1 = __builtin_amdgcn_exp2f(fmaf(sp[g >> 1][(2 * g + 1) & 3], SCALE_LOG2E, -mc));
      l += p0 + p1;
      const unsigned pk = pack_bf16(p0, p1);
      const u32x4 pa = {__builtin_amdgcn_perm(0u, pk, sel0), __builtin_amdgcn_perm(0u, pk, sel1), __builtin_amdgcn_perm(0u, pk, sel2),
                        __builtin_amdgcn_perm(0u, pk, sel3)};
      u32x4 blo[4], bhi[4];
#pragma unroll
      for (int mm = 0; mm < 4; ++mm) {
        const u32x4 a = ring[8 * g + 2 * mm], b = ring[8 * g + 2 * mm + 1];
#pragma unroll
        for (int wd = 0; wd < 4; ++wd) {
          blo[wd][mm] = __builtin_amdgcn_perm(b[wd], a[wd], 0x05040100u);
          bhi[wd][mm] = __builtin_amdgcn_perm(b[wd], a[wd], 0x07060302u);
        }
        __builtin_amdgcn_sched_barrier(0);
        ring[8 * g + 2 * mm] = ldnt(rv, voff, nbase + stream_off(8 * g + 2 * mm, 0));
        ring[8 * g + 2 * mm + 1] = ldnt(rv, voff, nbase + stream_off(8 * g + 2 * mm + 1, 0));
        __builtin_amdgcn_sched_barrier(0);
      }
#pragma unroll
      for (int wd = 0; wd < 4; ++wd) {
        acc[2 * wd] = mfma16(pa, blo[wd], acc[2 * wd]);
        acc[2 * wd + 1] = mfma16(pa, bhi[wd], acc[2 * wd + 1]);
      }
      __builtin_amdgcn_sched_barrier(0);
    }
  }
  STAMP(4);
  l += dpp<0xB1>(l);
  l += dpp<0x4E>(l);
  l = xrow_sum(l);
  float* rec = lds + LDS_SC + w * LDS_REC;
  {
    f32x4 o0, o1;
#pragma unroll
    for (int e = 0; e < 4; ++e) {
      o0[e] = (acc[e][0] + acc[e][1]) + (acc[e][2] + acc[e][3]);
      o1[e] = (acc[4 + e][0] + acc[4 + e][1]) + (acc[4 + e][2] + acc[4 + e][3]);
    }
    *(f32x4*)(rec + lane * 8) = o0;
    *(f32x4*)(rec + lane * 8 + 4) = o1;
    if ((lane & ~12) == 0) { rec[4 * D + (lane >> 2)] = mc; rec[4 * D + 4 + (lane >> 2)] = l; }
  }
  __syncthreads();
  STAMP(5);
  {
    const int split = blockIdx.x & 1, hh = tid >> 7, d = tid & 127;
    const float* r0 = lds + LDS_SC;
    float Mo, Lo, oo;
#pragma unroll
    for (int part = 0; part < 2; ++part) {
      const int h = 2 * (part == 0 ? 1 - split : split) + hh;
      float M = -INFINITY;
#pragma unroll
      for (int ww = 0; ww < NWAVE; ++ww) M = fmaxf(M, r0[ww * LDS_REC + 4 * D + h]);
      float L = 0.f, o = 0.f;
#pragma unroll
      for (int ww = 0; ww < NWAVE; ++ww) {
        const float f = __builtin_amdgcn_exp2f(r0[ww * LDS_REC + 4 * D + h] - M);
        L = fmaf(f, r0[ww * LDS_REC + 4 * D + 4 + h], L);
        o = fmaf(f, r0[ww * LDS_REC + h * D + d], o);
      }
      if (part == 0) {
        gu64* const pin = (gu64*)ws_pt;
        __hip_atomic_store(pin + tid, granule(o), RLX_AGENT);
        if (d < 2) __hip_atomic_store(pin + 256 + 2 * hh + d, granule(d == 0 ? M : L), RLX_AGENT);
      } else { Mo = M; Lo = L; oo = o; }
    }
    gu64* const inbox = (gu64*)ws_me;
    unsigned long long x0, x1, x2;
#pragma nounroll
    for (unsigned spins = 0; spins < SPIN_CAP; ++spins) {
      x0 = __hip_atomic_load(inbox + tid, RLX_AGENT);
      x1 = __hip_atomic_load(inbox + 256 + 2 * hh, RLX_AGENT);
      x2 = __hip_atomic_load(inbox + 257 + 2 * hh, RLX_AGENT);
      const bool ok = (unsigned)(x0 >> 32) == TAG && (unsigned)(x1 >> 32) == TAG && (unsigned)(x2 >> 32) == TAG;
      if (__all(ok)) break;
      __builtin_amdgcn_s_sleep(2);
    }
    const float oi = __uint_as_float((unsigned)x0), Mi = __uint_as_float((unsigned)x1), Li = __uint_as_float((unsigned)x2);
    const float M = fmaxf(Mo, Mi);
    const float fa = __builtin_amdgcn_exp2f(Mo - M), fb = __builtin_amdgcn_exp2f(Mi - M);
    const float res = (fa * oo + fb * oi) / (fa * Lo + fb * Li);
    out[((size_t)bh * G + 2 * split + hh) * D + d] = __builtin_bit_cast(unsigned short, (__bf16)res);
  }
  STAMP(6);
  STAMP_FLUSH();
}

}

extern "C" void kernel_launch(void* const* d_in, const int* in_sizes, int n_in, void* d_out, int out_size, void* d_ws,
                              size_t ws_size, hipStream_t stream) {
  using namespace gqa;
  attn_split<<<B * HKV * NSPLIT, NTHR, 0, stream>>>((const unsigned short*)d_in[0], (const unsigned short*)d_in[1],
                                                    (const unsigned short*)d_in[2], (char*)d_ws, (unsigned short*)d_out);
}
```

```cpp
#include <hip/hip_runtime.h>
#include <hip/hip_bf16.h>
#include <stdint.h>

namespace gqa {
#define WS_AT_END
#define STAMP(i)
#define STAMP_FLUSH()

constexpr int B = 16, HQ = 32, HKV = 8, SK = 4096, D = 128, G = HQ / HKV;
constexpr int NSPLIT = 2;
#define NWAVE 4
#define LOOK 5
#define LOOKV 6
#define H_START_DELAY 1
#define QMODE 1
#define VSINGLE 0
#define VSTAGE_OCT 3
#define HS_DELAY 1
constexpr int NTHR = (NWAVE + 1) * 64;
constexpr int KEYS_WG = SK / NSPLIT;
constexpr int NL = KEYS_WG / NWAVE / 4;
constexpr int NOCT = NL / 8;
constexpr unsigned WG_BYTES = KEYS_WG * D * 2;
constexpr float SCALE_LOG2E = 0.08838834764831845f * 1.4426950408889634f;

typedef unsigned u32x4 __attribute__((ext_vector_type(4)));
typedef float f32x4 __attribute__((ext_vector_type(4)));
typedef float f32x2 __attribute__((ext_vector_type(2)));
typedef __bf16 bf16x8 __attribute__((ext_vector_type(8)));
typedef __bf16 bf16x2 __attribute__((ext_vector_type(2)));
#define GAS __attribute__((address_space(1)))
#define LAS __attribute__((address_space(3)))
typedef GAS unsigned long long gu64;
typedef LAS volatile unsigned lds_vu32;
typedef LAS volatile float lds_vf32;
#define RLX_AGENT __ATOMIC_RELAXED, __HIP_MEMORY_SCOPE_AGENT

__device__ __forceinline__ u32x4 ldnt(__amdgpu_buffer_rsrc_t r, int voff, int soff) {
  return __builtin_amdgcn_raw_buffer_load_b128(r, voff, soff, 2  );
}
__device__ __forceinline__ f32x4 mfma16(u32x4 a, u32x4 b, f32x4 c) {
  return __builtin_amdgcn_mfma_f32_16x16x32_bf16(__builtin_bit_cast(bf16x8, a), __builtin_bit_cast(bf16x8, b), c, 0, 0, 0);
}
template <int CTRL> __device__ __forceinline__ float dpp(float x) {
  return __builtin_bit_cast(float, __builtin_amdgcn_mov_dpp(__builtin_bit_cast(int, x), CTRL, 0xf, 0xf, true));
}
__device__ __forceinline__ float xrow_max(float x) {
  auto s = __builtin_amdgcn_permlane16_swap(__float_as_uint(x), __float_as_uint(x), false, false);
  x = fmaxf(__uint_as_float(s[0]), __uint_as_float(s[1]));
  auto t = __builtin_amdgcn_permlane32_swap(__float_as_uint(x), __float_as_uint(x), false, false);
  return fmaxf(__uint_as_float(t[0]), __uint_as_float(t[1]));
}
__device__ __forceinline__ float xrow_sum(float x) {
  auto s = __builtin_amdgcn_permlane16_swap(__float_as_uint(x), __float_as_uint(x), false, false);
  x = __uint_as_float(s[0]) + __uint_as_float(s[1]);
  auto t = __builtin_amdgcn_permlane32_swap(__float_as_uint(x), __float_as_uint(x), false, false);
  return __uint_as_float(t[0]) + __uint_as_float(t[1]);
}
__device__ __forceinline__ unsigned pack_bf16(float lo, float hi) {
  bf16x2 r = {(__bf16)lo, (__bf16)hi};
  return __builtin_bit_cast(unsigned, r);
}
__device__ __forceinline__ unsigned lds_add_rtn(lds_vu32* p, int lane) {
  unsigned r;
  const unsigned inc = lane == 0 ? 1u : 0u, addr = (unsigned)(size_t)p;
  asm volatile("ds_add_rtn_u32 %0, %1, %2\n\ts_waitcnt lgkmcnt(0)" : "=v"(r) : "v"(addr), "v"(inc) : "memory");
  return __builtin_amdgcn_readfirstlane(r);
}
constexpr int ld_off(int j) { return ((j >> 2) * (4 * NWAVE) + (j & 3)) * 1024; }
constexpr int OCT_BYTES = 8 * NWAVE * 1024;

constexpr int WS_WG = 8192, WS_NONCE = 6144, WS_ECHO = 7168, WS_ROLE = 7680;
constexpr int INBOX_BYTES = 4096 + 64;
constexpr unsigned SPIN_CAP = 1u << 20;
__device__ __forceinline__ u32x4 granules(float a, float b, unsigned long long tag40) {
  const unsigned lo = (unsigned)tag40 << 24, hi = (unsigned)(tag40 >> 8);
  return u32x4{lo | (__float_as_uint(a) >> 8), hi, lo | (__float_as_uint(b) >> 8), hi};
}
__device__ __forceinline__ unsigned granules_bad(u32x4 g, unsigned long long tag40) {
  const unsigned lo = (unsigned)tag40 & 0xffu, hi = (unsigned)(tag40 >> 8);
  return ((g[0] >> 24) ^ lo) | (g[1] ^ hi) | ((g[2] >> 24) ^ lo) | (g[3] ^ hi);
}
__device__ __forceinline__ float gran_a(u32x4 g) { return __uint_as_float(g[0] << 8); }
__device__ __forceinline__ float gran_b(u32x4 g) { return __uint_as_float(g[2] << 8); }
constexpr int AUX_SC1 = 16;

constexpr int LDS_SC = NWAVE * NOCT * 2 * 64;
constexpr int LDS_REC = 4 * D + 4;
constexpr int LDS_MW = LDS_SC + NWAVE * LDS_REC;
constexpr int LDS_PM = LDS_MW + NWAVE * 4;
constexpr int LDS_CTL = LDS_PM + 4 * D + 8;
enum { C_CNT = 0, C_PFLAG = 1, C_ROLE = 2, C_VSTAGE = 3, C_DONE = 4, C_KDONE = 5, C_TAIL = 6, C_TAGME = 8, C_TAGPT = 10, C_WORDS = 16 };

template <bool LAST>
__device__ __forceinline__ void v_octet(u32x4 (&ring)[8], f32x4 (&acc)[8], float& l, const f32x2 sp, const float mc, const unsigned sel0, const unsigned sel1,
                                        const unsigned sel2, const unsigned sel3, const __amdgpu_buffer_rsrc_t rv, const int voff, const int cbase) {
  const float p0 = __builtin_amdgcn_exp2f(fmaf(sp[0], SCALE_LOG2E, -mc));
  const float p1 = __builtin_amdgcn_exp2f(fmaf(sp[1], SCALE_LOG2E, -mc));
  l += p0 + p1;
  const unsigned pk = pack_bf16(p0, p1);
  const u32x4 pa = {__builtin_amdgcn_perm(0u, pk, sel0), __builtin_amdgcn_perm(0u, pk, sel1), __builtin_amdgcn_perm(0u, pk, sel2),
                    __builtin_amdgcn_perm(0u, pk, sel3)};
  u32x4 blo[4], bhi[4];
#pragma unroll
  for (int mm = 0; mm < 4; ++mm) {
    const u32x4 a = ring[2 * mm], b = ring[2 * mm + 1];
    asm volatile("" :: "v"(a[0]), "v"(b[0]) : "memory");
    __builtin_amdgcn_sched_barrier(0);
#pragma unroll
    for (int u = 0; u < 2; ++u) {
      const int t = 2 * mm + u + LOOKV;
      if (LOOKV + u <= 7) { if (!LAST || t < 8) ring[t & 7] = ldnt(rv, voff, cbase + (t < 8 ? ld_off(t) : OCT_BYTES + ld_off(t - 8))); }
    }
    __builtin_amdgcn_sched_barrier(0);
#pragma unroll
    for (int wd = 0; wd < 4; ++wd) {
      blo[wd][mm] = __builtin_amdgcn_perm(b[wd], a[wd], 0x05040100u);
      bhi[wd][mm] = __builtin_amdgcn_perm(b[wd], a[wd], 0x07060302u);
    }
    asm volatile("" : "+v"(blo[0][mm]), "+v"(blo[1][mm]), "+v"(blo[2][mm]), "+v"(blo[3][mm]), "+v"(bhi[0][mm]), "+v"(bhi[1][mm]), "+v"(bhi[2][mm]),
                 "+v"(bhi[3][mm]) :: "memory");
    __builtin_amdgcn_sched_barrier(0);
#pragma unroll
    for (int u = 0; u < 2; ++u) {
      const int t = 2 * mm + u + LOOKV;
      if (VSINGLE ? (LOOKV + u > 7 - (1 - u)) : (LOOKV + u > 7)) { if (!LAST || t < 8) ring[t & 7] = ldnt(rv, voff, cbase + (t < 8 ? ld_off(t) : OCT_BYTES + ld_off(t - 8))); }
    }
    __builtin_amdgcn_sched_barrier(0);
  }
#pragma unroll
  for (int wd = 0; wd < 4; ++wd) {
    acc[2 * wd] = mfma16(pa, blo[wd], acc[2 * wd]);
    acc[2 * wd + 1] = mfma16(pa, bhi[wd], acc[2 * wd + 1]);
  }
  __builtin_amdgcn_sched_barrier(0);
}

__global__ void __launch_bounds__(NTHR, 2)
attn_split(const unsigned short* __restrict__ q, const unsigned short* __restrict__ k, const unsigned short* __restrict__ v,
           char* __restrict__ ws, unsigned short* __restrict__ out) {
  __shared__ __attribute__((aligned(16))) float lds[LDS_CTL + C_WORDS];
  STAMP(0);
  asm volatile("" :: "s"(q), "s"(k), "s"(v), "s"(ws), "s"(out));
  const int tid = threadIdx.x, lane = tid & 63;
  const int w = __builtin_amdgcn_readfirstlane(tid >> 6);
  const int bh = blockIdx.x >> 1;
  GAS char* const ws_me = (GAS char*)ws + (size_t)blockIdx.x * WS_WG;
  GAS char* const ws_pt = (GAS char*)ws + (size_t)(blockIdx.x ^ 1) * WS_WG;
  lds_vu32* const ctl = (lds_vu32*)(LAS float*)lds + LDS_CTL;
  if (w == NWAVE) {
    if (lane < C_WORDS) ctl[lane] = 0u;
    asm volatile("s_waitcnt lgkmcnt(0)" ::: "memory");
    __builtin_amdgcn_s_barrier();
    gu64* const my_nonce = (gu64*)(ws_me + WS_NONCE);
    gu64* const my_echo = (gu64*)(ws_me + WS_ECHO);
    gu64* const pt_nonce = (gu64*)(ws_pt + WS_NONCE);
    gu64* const pt_echo = (gu64*)(ws_pt + WS_ECHO);
#pragma unroll
    for (int i = 0; i < H_START_DELAY; ++i) __builtin_amdgcn_s_sleep(127);
    const unsigned long long nonce = __hip_atomic_load(my_nonce, RLX_AGENT) + 1ull;
    if (lane == 0) __hip_atomic_store(my_nonce, nonce, RLX_AGENT);
    asm volatile("s_waitcnt vmcnt(0)" ::: "memory");
#pragma unroll
    for (int i = 0; i < HS_DELAY; ++i) __builtin_amdgcn_s_sleep(127);
    unsigned long long echoed = nonce;
    bool first = true;
#pragma nounroll
    for (unsigned spins = 0; spins < SPIN_CAP; ++spins) {
      const unsigned long long e = __hip_atomic_load(pt_nonce, RLX_AGENT);
      const unsigned long long r = __hip_atomic_load(pt_echo, RLX_AGENT);
      if (__builtin_amdgcn_readfirstlane((int)(first || e != echoed))) { if (lane == 0) __hip_atomic_store(my_echo, e, RLX_AGENT); echoed = e; first = false; }
      if (__builtin_amdgcn_readfirstlane((int)(r == nonce))) break;
      __builtin_amdgcn_s_sleep(16);
    }
    const unsigned long long pt = __hip_atomic_load(pt_nonce, RLX_AGENT);
    if (__builtin_amdgcn_readfirstlane((int)(pt != echoed))) { if (lane == 0) __hip_atomic_store(my_echo, pt, RLX_AGENT); }
    if (lane == 0) { ctl[C_TAGME] = (unsigned)nonce; ctl[C_TAGME + 1] = (unsigned)(nonce >> 32); ctl[C_TAGPT] = (unsigned)pt; ctl[C_TAGPT + 1] = (unsigned)(pt >> 32); }
    while (ctl[C_VSTAGE] == 0u) __builtin_amdgcn_s_sleep(16);
    unsigned long long old = 0;
    if (lane == 0) old = __hip_atomic_exchange((gu64*)(((blockIdx.x & 1) ? ws_pt : ws_me) + WS_ROLE), nonce, RLX_AGENT);
    const bool finisher = __builtin_amdgcn_readfirstlane((int)(old == pt)) != 0;
    if (lane == 0) ctl[C_ROLE] = finisher ? 2u : 1u;
    if (!finisher) return;
    const __amdgpu_buffer_rsrc_t rin = __builtin_amdgcn_make_buffer_rsrc((void*)ws_me, 0, INBOX_BYTES, 0x00020000);
    while (ctl[C_TAIL] == 0u) __builtin_amdgcn_s_sleep(4);
#pragma nounroll
    for (unsigned spins = 0; spins < SPIN_CAP; ++spins) {
      if (ctl[C_DONE] != 0u) break;
      asm volatile("" ::: "memory");
      u32x4 g[5];
#pragma unroll
      for (int e = 0; e < 4; ++e) g[e] = __builtin_amdgcn_raw_buffer_load_b128(rin, e * 1024 + lane * 16, 0, AUX_SC1);
      g[4] = __builtin_amdgcn_raw_buffer_load_b128(rin, 4096 + (lane >> 4) * 16, 0, AUX_SC1);
      unsigned bad = 0;
#pragma unroll
      for (int e = 0; e < 5; ++e) bad |= granules_bad(g[e], nonce & 0xffffffffffull);
      if (__all(bad == 0u)) {
        float* pm = lds + LDS_PM;
        *(f32x4*)(pm + lane * 8) = f32x4{gran_a(g[0]), gran_b(g[0]), gran_a(g[1]), gran_b(g[1])};
        *(f32x4*)(pm + lane * 8 + 4) = f32x4{gran_a(g[2]), gran_b(g[2]), gran_a(g[3]), gran_b(g[3])};
        if ((lane & 15) == 0) { pm[4 * D + (lane >> 4)] = gran_a(g[4]); pm[4 * D + 4 + (lane >> 4)] = gran_b(g[4]); }
        asm volatile("s_waitcnt lgkmcnt(0)" ::: "memory");
        if (lane == 0) ctl[C_PFLAG] = 1u;
        break;
      }
    }
    return;
  }
  const __amdgpu_buffer_rsrc_t rk = __builtin_amdgcn_make_buffer_rsrc((void*)(k + (size_t)blockIdx.x * (KEYS_WG * D)), 0, WG_BYTES, 0x00020000);
  const __amdgpu_buffer_rsrc_t rv = __builtin_amdgcn_make_buffer_rsrc((void*)(v + (size_t)blockIdx.x * (KEYS_WG * D)), 0, WG_BYTES, 0x00020000);
  const int koff = ((lane >> 2) & 3) * 256 + (lane >> 4) * 64 + (lane & 3) * 16;
  const int voff = lane * 16;
  const int p = lane & 3;
  const int wbase = w * 4096;

  const u32x4 qf = __builtin_nontemporal_load((const GAS u32x4*)((const GAS char*)q + (size_t)bh * (G * D * 2) + koff));
  __builtin_amdgcn_sched_barrier(0);
  u32x4 ring[8];
#pragma unroll
  for (int i = 0; i < LOOK; ++i) { ring[i] = ldnt(rk, koff, wbase + ld_off(i)); __builtin_amdgcn_sched_barrier(0); }
  __builtin_amdgcn_s_barrier();
  STAMP(1);

  f32x2* sc = (f32x2*)lds + w * NOCT * 64 + lane;
  float m = -INFINITY;
#pragma nounroll
  for (int it = 0; it < NOCT; ++it) {
    const bool last = it == NOCT - 1;
    const __amdgpu_buffer_rsrc_t rn = last ? rv : rk;
    const int noff = last ? voff : koff;
    const int cbase = wbase + it * OCT_BYTES;
    const int nbase = last ? wbase : cbase + OCT_BYTES;
    f32x2 sp;
#pragma unroll
    for (int n = 0; n < 2; ++n) {
      f32x4 d[4];
#pragma unroll
      for (int j = 0; j < 4; ++j) {
        const int i = 4 * n + j, t = i + LOOK;
        d[j] = mfma16(ring[i], qf, f32x4{0.f, 0.f, 0.f, 0.f});
        __builtin_amdgcn_sched_barrier(0);
        ring[t & 7] = t < 8 ? ldnt(rk, koff, cbase + ld_off(t)) : ldnt(rn, noff, nbase + ld_off(t - 8));
        __builtin_amdgcn_sched_barrier(0);
      }
      float s[4];
#pragma unroll
      for (int j = 0; j < 4; ++j) s[j] = dpp<0x00>(d[j][0]);
#pragma unroll
      for (int j = 0; j < 4; ++j) s[j] += dpp<0x55>(d[j][1]);
#pragma unroll
      for (int j = 0; j < 4; ++j) s[j] += dpp<0xAA>(d[j][2]);
#pragma unroll
      for (int j = 0; j < 4; ++j) { s[j] += dpp<0xFF>(d[j][3]); asm("" : "+v"(s[j])); }
      float sv = s[0];
      sv = p == 1 ? s[1] : sv;
      sv = p == 2 ? s[2] : sv;
      sv = p == 3 ? s[3] : sv;
      m = fmaxf(m, sv);
      sp[n] = sv;
      __builtin_amdgcn_sched_barrier(0);
    }
    sc[it * 64] = sp;
  }
#pragma unroll
  for (int i = LOOK; i < LOOKV; ++i) ring[i & 7] = ldnt(rv, voff, wbase + (i < 8 ? ld_off(i) : OCT_BYTES + ld_off(i - 8)));
  STAMP(3);
  m = fmaxf(m, dpp<0xB1>(m));
  m = fmaxf(m, dpp<0x4E>(m));
  m = xrow_max(m);
  const float mc = m * SCALE_LOG2E;
  lds_vf32* const mw = (lds_vf32*)(LAS float*)lds + LDS_MW;
  if ((lane & ~12) == 0) mw[w * 4 + (lane >> 2)] = mc;
  (void)lds_add_rtn(ctl + C_KDONE, lane);
  const unsigned lo_sel = (p & 1) ? 0x01000c0cu : 0x0c0c0100u, hi_sel = (p & 1) ? 0x03020c0cu : 0x0c0c0302u, zsel = 0x0c0c0c0cu;
  const unsigned sel0 = p < 2 ? lo_sel : zsel, sel1 = p < 2 ? zsel : lo_sel, sel2 = p < 2 ? hi_sel : zsel, sel3 = p < 2 ? zsel : hi_sel;
  float l = 0.f;
  f32x4 acc[8];
#pragma unroll
  for (int e = 0; e < 8; ++e) acc[e] = f32x4{0.f, 0.f, 0.f, 0.f};
#pragma nounroll
  for (int it = 0; it < NOCT - 1; ++it) {
    if (it == NOCT - VSTAGE_OCT && w == 0) ctl[C_VSTAGE] = 1u;
    v_octet<false>(ring, acc, l, sc[it * 64], mc, sel0, sel1, sel2, sel3, rv, voff, wbase + it * OCT_BYTES);
  }
  if (w == 0) ctl[C_TAIL] = 1u;
  const int h = lane >> 4, hs = (lane >> 2) & 3;
  float Mo, Ms, mo_own;
  for (;;) {
    const unsigned kd = ctl[C_KDONE];
    float a[NWAVE], b[NWAVE];
#pragma unroll
    for (int ww = 0; ww < NWAVE; ++ww) { a[ww] = mw[ww * 4 + h]; b[ww] = mw[ww * 4 + hs]; }
    mo_own = mw[w * 4 + h];
    Mo = a[0]; Ms = b[0];
#pragma unroll
    for (int ww = 1; ww < NWAVE; ++ww) { Mo = fmaxf(Mo, a[ww]); Ms = fmaxf(Ms, b[ww]); }
    if (__builtin_amdgcn_readfirstlane(kd) == NWAVE) break;
    __builtin_amdgcn_s_sleep(1);
  }
  const float fo = __builtin_amdgcn_exp2f(mo_own - Mo), fs = __builtin_amdgcn_exp2f(mc - Ms);
  v_octet<true>(ring, acc, l, sc[(NOCT - 1) * 64], mc, sel0, sel1, sel2, sel3, rv, voff, wbase + (NOCT - 1) * OCT_BYTES);
  STAMP(4);
  l += dpp<0xB1>(l);
  l += dpp<0x4E>(l);
  l = xrow_sum(l) * fs;
  float* rec = lds + LDS_SC + w * LDS_REC;
  {
    f32x4 o0, o1;
#pragma unroll
    for (int e = 0; e < 4; ++e) {
      o0[e] = ((acc[e][0] + acc[e][1]) + (acc[e][2] + acc[e][3])) * fo;
      o1[e] = ((acc[4 + e][0] + acc[4 + e][1]) + (acc[4 + e][2] + acc[4 + e][3])) * fo;
    }
    *(f32x4*)(rec + lane * 8) = o0;
    *(f32x4*)(rec + lane * 8 + 4) = o1;
    if ((lane & ~12) == 0) rec[4 * D + (lane >> 2)] = l;
  }
  const unsigned arrived = lds_add_rtn(ctl + C_CNT, lane);
  STAMP(5);
  if (arrived == NWAVE - 1) {
    const int split = blockIdx.x & 1;
    const float* r0 = lds + LDS_SC;
    const unsigned have = ctl[C_PFLAG];
    unsigned role = ctl[C_ROLE];
    float L = 0.f;
    f32x4 o0 = {0.f, 0.f, 0.f, 0.f}, o1 = {0.f, 0.f, 0.f, 0.f};
#pragma unroll
    for (int ww = 0; ww < NWAVE; ++ww) {
      L += r0[ww * LDS_REC + 4 * D + h];
      o0 += *(const f32x4*)(r0 + ww * LDS_REC + lane * 8);
      o1 += *(const f32x4*)(r0 + ww * LDS_REC + lane * 8 + 4);
    }
    while (role == 0u) { __builtin_amdgcn_s_sleep(1); role = ctl[C_ROLE]; }
    if (__builtin_amdgcn_readfirstlane(role) == 1u) {
      const unsigned long long tg = (((unsigned long long)ctl[C_TAGPT + 1] << 32) | ctl[C_TAGPT]) & 0xffffffffffull;
      const __amdgpu_buffer_rsrc_t rout = __builtin_amdgcn_make_buffer_rsrc((void*)ws_pt, 0, INBOX_BYTES, 0x00020000);
      __builtin_amdgcn_raw_buffer_store_b128(granules(o0[0], o0[1], tg), rout, lane * 16, 0, AUX_SC1);
      __builtin_amdgcn_raw_buffer_store_b128(granules(o0[2], o0[3], tg), rout, 1024 + lane * 16, 0, AUX_SC1);
      __builtin_amdgcn_raw_buffer_store_b128(granules(o1[0], o1[1], tg), rout, 2048 + lane * 16, 0, AUX_SC1);
      __builtin_amdgcn_raw_buffer_store_b128(granules(o1[2], o1[3], tg), rout, 3072 + lane * 16, 0, AUX_SC1);
      if ((lane & 15) == 0) __builtin_amdgcn_raw_buffer_store_b128(granules(Mo, L, tg), rout, 4096 + h * 16, 0, AUX_SC1);
    } else {
      float Mi, Li;
      f32x4 i0, i1;
      if (__builtin_amdgcn_readfirstlane(have)) {
        const float* pm = lds + LDS_PM;
        i0 = *(const f32x4*)(pm + lane * 8); i1 = *(const f32x4*)(pm + lane * 8 + 4);
        Mi = pm[4 * D + h]; Li = pm[4 * D + 4 + h];
      } else {
        const unsigned long long tg = (((unsigned long long)ctl[C_TAGME + 1] << 32) | ctl[C_TAGME]) & 0xffffffffffull;
        const __amdgpu_buffer_rsrc_t rin = __builtin_amdgcn_make_buffer_rsrc((void*)ws_me, 0, INBOX_BYTES, 0x00020000);
#pragma nounroll
        for (unsigned spins = 0; spins < SPIN_CAP; ++spins) {
          asm volatile("" ::: "memory");
          u32x4 g[5];
#pragma unroll
          for (int e = 0; e < 4; ++e) g[e] = __builtin_amdgcn_raw_buffer_load_b128(rin, e * 1024 + lane * 16, 0, AUX_SC1);
          g[4] = __builtin_amdgcn_raw_buffer_load_b128(rin, 4096 + h * 16, 0, AUX_SC1);
          unsigned bad = 0;
#pragma unroll
          for (int e = 0; e < 5; ++e) bad |= granules_bad(g[e], tg);
          i0 = f32x4{gran_a(g[0]), gran_b(g[0]), gran_a(g[1]), gran_b(g[1])};
          i1 = f32x4{gran_a(g[2]), gran_b(g[2]), gran_a(g[3]), gran_b(g[3])};
          Mi = gran_a(g[4]); Li = gran_b(g[4]);
          if (__all(bad == 0u)) break;
        }
      }
      if (lane == 0) ctl[C_DONE] = 1u;
      const float Ma = split ? Mi : Mo, Mb = split ? Mo : Mi, La = split ? Li : L, Lb = split ? L : Li;
      const float Mx = fmaxf(Ma, Mb);
      const float fa = __builtin_amdgcn_exp2f(Ma - Mx), fb = __builtin_amdgcn_exp2f(Mb - Mx);
      const float inv = 1.0f / fmaf(fa, La, fb * Lb);
      const float ga = fa * inv, gb = fb * inv;
      float r[8];
#pragma unroll
      for (int e = 0; e < 4; ++e) {
        r[e] = fmaf(ga, split ? i0[e] : o0[e], gb * (split ? o0[e] : i0[e]));
        r[4 + e] = fmaf(ga, split ? i1[e] : o1[e], gb * (split ? o1[e] : i1[e]));
      }
      const u32x4 pk4 = {pack_bf16(r[0], r[1]), pack_bf16(r[2], r[3]), pack_bf16(r[4], r[5]), pack_bf16(r[6], r[7])};
      *(GAS u32x4*)((GAS char*)out + (size_t)bh * (G * D * 2) + lane * 16) = pk4;
    }
  }
  STAMP(6);
  STAMP_FLUSH();
}

}

#define WS_SHIFT(sz) (((sz) - (size_t)B * HKV * NSPLIT * WS_WG) & ~(size_t)4095)
extern "C" void kernel_launch(void* const* d_in, const int* in_sizes, int n_in, void* d_out, int out_size, void* d_ws,
                              size_t ws_size, hipStream_t stream) {
  using namespace gqa;
  attn_split<<<B * HKV * NSPLIT, NTHR, 0, stream>>>((const unsigned short*)d_in[0], (const unsigned short*)d_in[1],
                                                    (const unsigned short*)d_in[2], (char*)d_ws + WS_SHIFT(ws_size), (unsigned short*)d_out);
}
```

```cpp
#include <hip/hip_runtime.h>
#include <hip/hip_bf16.h>
#include <stdint.h>

namespace gqa {
#define STAMP(i)
#define STAMP_FLUSH()

constexpr int B = 16, HQ = 32, HKV = 8, SK = 4096, D = 128, G = HQ / HKV;
constexpr int NSPLIT = 2;
constexpr int NWG = B * HKV * NSPLIT;
constexpr int NWAVE = 4;
#define WN 5
#define WW 6
#define H_START_DELAY 1
#define HS_DELAY 1
#define ZAUX 18
#define NDUMMY 0
#define MPER 8
#define DUTY_PAT 0x00u
#define STARTUP_TICKS 90
#define SCHED_BIAS 20
#define PACE_TRIES 3
#define PACE_POLL 8
#define PACE_WAIT 24
constexpr int NTHR = (NWAVE + 1) * 64;
constexpr int KEYS_WG = SK / NSPLIT;
constexpr int NL = KEYS_WG / NWAVE / 4;
constexpr int NOCT = NL / 8;
constexpr int NMILE = 2 * NOCT / MPER - 1;
constexpr int LPRE = 4;
constexpr unsigned WG_BYTES = KEYS_WG * D * 2;
constexpr float SCALE_LOG2E = 0.08838834764831845f * 1.4426950408889634f;

typedef unsigned u32x4 __attribute__((ext_vector_type(4)));
typedef float f32x4 __attribute__((ext_vector_type(4)));
typedef float f32x2 __attribute__((ext_vector_type(2)));
typedef __bf16 bf16x8 __attribute__((ext_vector_type(8)));
typedef __bf16 bf16x2 __attribute__((ext_vector_type(2)));
#define GAS __attribute__((address_space(1)))
#define LAS __attribute__((address_space(3)))
typedef GAS unsigned long long gu64;
typedef LAS volatile unsigned lds_vu32;
typedef LAS volatile float lds_vf32;
#define RLX_AGENT __ATOMIC_RELAXED, __HIP_MEMORY_SCOPE_AGENT
constexpr int AUX_SC1 = 16;

__device__ __forceinline__ f32x4 mfma16(u32x4 a, u32x4 b, f32x4 c) {
  return __builtin_amdgcn_mfma_f32_16x16x32_bf16(__builtin_bit_cast(bf16x8, a), __builtin_bit_cast(bf16x8, b), c, 0, 0, 0);
}
template <int CTRL> __device__ __forceinline__ float dpp(float x) {
  return __builtin_bit_cast(float, __builtin_amdgcn_mov_dpp(__builtin_bit_cast(int, x), CTRL, 0xf, 0xf, true));
}
__device__ __forceinline__ float xrow_max(float x) {
  auto s = __builtin_amdgcn_permlane16_swap(__float_as_uint(x), __float_as_uint(x), false, false);
  x = fmaxf(__uint_as_float(s[0]), __uint_as_float(s[1]));
  auto t = __builtin_amdgcn_permlane32_swap(__float_as_uint(x), __float_as_uint(x), false, false);
  return fmaxf(__uint_as_float(t[0]), __uint_as_float(t[1]));
}
__device__ __forceinline__ float xrow_sum(float x) {
  auto s = __builtin_amdgcn_permlane16_swap(__float_as_uint(x), __float_as_uint(x), false, false);
  x = __uint_as_float(s[0]) + __uint_as_float(s[1]);
  auto t = __builtin_amdgcn_permlane32_swap(__float_as_uint(x), __float_as_uint(x), false, false);
  return __uint_as_float(t[0]) + __uint_as_float(t[1]);
}
__device__ __forceinline__ unsigned pack_bf16(float lo, float hi) {
  bf16x2 r = {(__bf16)lo, (__bf16)hi};
  return __builtin_bit_cast(unsigned, r);
}
__device__ __forceinline__ unsigned lds_add_rtn(lds_vu32* p, int lane) {
  unsigned r;
  const unsigned inc = lane == 0 ? 1u : 0u, addr = (unsigned)(size_t)p;
  asm volatile("ds_add_rtn_u32 %0, %1, %2\n\ts_waitcnt lgkmcnt(0)" : "=v"(r) : "v"(addr), "v"(inc) : "memory");
  return __builtin_amdgcn_readfirstlane(r);
}
__device__ __forceinline__ void lds_add(lds_vu32* p, int lane) {
  const unsigned inc = lane == 0 ? 1u : 0u, addr = (unsigned)(size_t)p;
  asm volatile("ds_add_u32 %0, %1" :: "v"(addr), "v"(inc) : "memory");
}
constexpr int ld_off(int j) { return ((j >> 2) * (4 * NWAVE) + (j & 3)) * 1024; }
constexpr int OCT_BYTES = 8 * NWAVE * 1024;

constexpr int WS_WG = 1024, WS_INBOX = 0, WS_NONCE = 256, WS_ECHO = 512, WS_TIME = 768;
constexpr unsigned SPIN_CAP = 1u << 20;
__device__ __forceinline__ u32x4 granules(float a, float b, unsigned long long tag40) {
  const unsigned lo = (unsigned)tag40 << 24, hi = (unsigned)(tag40 >> 8);
  return u32x4{lo | (__float_as_uint(a) >> 8), hi, lo | (__float_as_uint(b) >> 8), hi};
}
__device__ __forceinline__ unsigned granules_bad(u32x4 g, unsigned long long tag40) {
  const unsigned lo = (unsigned)tag40 & 0xffu, hi = (unsigned)(tag40 >> 8);
  return ((g[0] >> 24) ^ lo) | (g[1] ^ hi) | ((g[2] >> 24) ^ lo) | (g[3] ^ hi);
}
__device__ __forceinline__ float cut24(float x) { return __uint_as_float(__float_as_uint(x) & 0xffffff00u); }
__device__ __forceinline__ float gran_a(u32x4 g) { return __uint_as_float(g[0] << 8); }
__device__ __forceinline__ float gran_b(u32x4 g) { return __uint_as_float(g[2] << 8); }

constexpr int LDS_SC = NWAVE * NOCT * 2 * 64;
constexpr int LDS_REC = 4 * D;
constexpr int LDS_MW = LDS_SC + NWAVE * LDS_REC;
constexpr int LDS_LW = LDS_MW + NWAVE * 4;
constexpr int LDS_NM = LDS_LW + NWAVE * 4;
constexpr int LDS_CTL = LDS_NM + 8;
enum { C_CNT = 0, C_LDONE = 1, C_NORM = 2, C_MILE = 3, C_SCHED = 4  , C_MT = 8  , C_WORDS = 16 };

__device__ __forceinline__ void first_load(u32x4& x, __amdgpu_buffer_rsrc_t r, int voff, int soff) {
  asm volatile("buffer_load_dwordx4 %0, %1, %2, %3 offen nt" : "=v"(x) : "v"(voff), "s"(r), "s"(soff) : "memory");
}
__device__ __forceinline__ void next_load(u32x4& x, u32x4& a, u32x4& b, unsigned wide, __amdgpu_buffer_rsrc_t r, int voff, int soff) {
  asm volatile("s_cmp_lg_u32 %3, 0\n\ts_cbranch_scc1 1f\n\t"
               "s_waitcnt vmcnt(%7)\n\tbuffer_load_dwordx4 %0, %4, %5, %6 offen nt\n\ts_branch 2f\n"
               "1:\n\ts_waitcnt vmcnt(%8)\n\tbuffer_load_dwordx4 %0, %4, %5, %6 offen nt\n"
               "2:"
               : "=&v"(x), "+v"(a), "+v"(b) : "s"(wide), "v"(voff), "s"(r), "s"(soff), "n"(WN - 1), "n"(WW - 1) : "scc", "memory");
}
__device__ __forceinline__ void narrow_pad(unsigned&, unsigned, __amdgpu_buffer_rsrc_t, int) {}
template <int N> __device__ __forceinline__ void wait_loads(u32x4& a, u32x4& b) {
  asm volatile("s_waitcnt vmcnt(%2)" : "+v"(a), "+v"(b) : "n"(N) : "memory");
}
static_assert(WN >= 2 && WN <= WW && WW <= 7, "window");

template <bool LAST, class Tail>
__device__ __forceinline__ void v_octet(u32x4 (&ring)[8], f32x4 (&acc)[8], const f32x2 sp, const float mc, const unsigned sel0, const unsigned sel1,
                                        const unsigned sel2, const unsigned sel3, const __amdgpu_buffer_rsrc_t rv, const __amdgpu_buffer_rsrc_t rz, const int voff, const int cbase,
                                        const unsigned wide, unsigned& junk, Tail&& tail) {
  u32x4 blo[4], bhi[4], pa;
#pragma unroll
  for (int mm = 0; mm < 4; ++mm) {
    if (!LAST || mm == 0) {
      next_load(ring[(2 * mm + 7) & 7], ring[2 * mm], ring[2 * mm + 1], wide, rv, voff, cbase + (mm == 0 ? ld_off(7) : OCT_BYTES + ld_off(2 * mm + 7 - 8)));
      if (WW > 6) wait_loads<6>(ring[2 * mm], ring[2 * mm + 1]);
    }
    else if (mm == 1) wait_loads<4>(ring[2], ring[3]);
    else if (mm == 2) wait_loads<2>(ring[4], ring[5]);
    else wait_loads<0>(ring[6], ring[7]);
    __builtin_amdgcn_sched_barrier(0);
    const u32x4 a = ring[2 * mm], b = ring[2 * mm + 1];
#pragma unroll
    for (int wd = 0; wd < 4; ++wd) {
      blo[wd][mm] = __builtin_amdgcn_perm(b[wd], a[wd], 0x05040100u);
      bhi[wd][mm] = __builtin_amdgcn_perm(b[wd], a[wd], 0x07060302u);
    }
    asm volatile("" : "+v"(blo[0][mm]), "+v"(blo[1][mm]), "+v"(blo[2][mm]), "+v"(blo[3][mm]), "+v"(bhi[0][mm]), "+v"(bhi[1][mm]), "+v"(bhi[2][mm]),
                 "+v"(bhi[3][mm]) :: "memory");
    __builtin_amdgcn_sched_barrier(0);
    if (!LAST) {
      next_load(ring[2 * mm], ring[(2 * mm + 2) & 7], ring[(2 * mm + 3) & 7], wide, rv, voff, cbase + OCT_BYTES + ld_off(2 * mm + 8 - 8));
      __builtin_amdgcn_sched_barrier(0);
      if ((NDUMMY >= 1 && mm == 0) || (NDUMMY >= 2 && mm == 2)) { narrow_pad(junk, wide, rz, voff); __builtin_amdgcn_sched_barrier(0); }
    }
    if (mm == 0) {
      const float p0 = __builtin_amdgcn_exp2f(fmaf(sp[0], SCALE_LOG2E, -mc));
      const float p1 = __builtin_amdgcn_exp2f(fmaf(sp[1], SCALE_LOG2E, -mc));
      const unsigned pk = pack_bf16(p0, p1);
      pa = u32x4{__builtin_amdgcn_perm(0u, pk, sel0), __builtin_amdgcn_perm(0u, pk, sel1), __builtin_amdgcn_perm(0u, pk, sel2),
                 __builtin_amdgcn_perm(0u, pk, sel3)};
      asm volatile("" : "+v"(pa));
      __builtin_amdgcn_sched_barrier(0);
    }
  }
  tail();
  __builtin_amdgcn_sched_barrier(0);
#pragma unroll
  for (int wd = 0; wd < 4; ++wd) {
    acc[2 * wd] = mfma16(pa, blo[wd], acc[2 * wd]);
    acc[2 * wd + 1] = mfma16(pa, bhi[wd], acc[2 * wd + 1]);
  }
  __builtin_amdgcn_sched_barrier(0);
}

__global__ void __launch_bounds__(NTHR, 2)
attn_split(const unsigned short* __restrict__ q, const unsigned short* __restrict__ k, const unsigned short* __restrict__ v,
           char* __restrict__ ws, unsigned short* __restrict__ out) {
  __shared__ __attribute__((aligned(16))) float lds[LDS_CTL + C_WORDS];
  STAMP(0);
  asm volatile("" :: "s"(q), "s"(k), "s"(v), "s"(ws), "s"(out));
  const int tid = threadIdx.x, lane = tid & 63;
  const int w = __builtin_amdgcn_readfirstlane(tid >> 6);
  const int cid = blockIdx.x, bh = cid >> 1;
  lds_vu32* const ctl = (lds_vu32*)(LAS float*)lds + LDS_CTL;
  lds_vf32* const mw = (lds_vf32*)(LAS float*)lds + LDS_MW;
  lds_vf32* const lw = (lds_vf32*)(LAS float*)lds + LDS_LW;
  lds_vf32* const nm = (lds_vf32*)(LAS float*)lds + LDS_NM;
  GAS char* const out_row = (GAS char*)out + (size_t)bh * (G * D * 2);
  if (w == NWAVE) {
    if (lane < C_WORDS) ctl[lane] = 0u;
    const unsigned long long t_entry = __builtin_amdgcn_s_memrealtime();
    asm volatile("s_waitcnt lgkmcnt(0)" ::: "memory");
    __builtin_amdgcn_s_barrier();
    GAS char* const ws_me = (GAS char*)ws + (size_t)cid * WS_WG;
    GAS char* const ws_pt = (GAS char*)ws + (size_t)(cid ^ 1) * WS_WG;
    gu64* const my_nonce = (gu64*)(ws_me + WS_NONCE);
    gu64* const my_echo = (gu64*)(ws_me + WS_ECHO);
    gu64* const pt_nonce = (gu64*)(ws_pt + WS_NONCE);
    gu64* const pt_echo = (gu64*)(ws_pt + WS_ECHO);
#pragma unroll
    for (int i = 0; i < H_START_DELAY; ++i) __builtin_amdgcn_s_sleep(127);
    const unsigned long long t_start = __builtin_amdgcn_s_memrealtime();
    const unsigned long long nonce = __hip_atomic_load(my_nonce, RLX_AGENT) + 1ull;
    if (!(cid & 1)) {
      const __amdgpu_buffer_rsrc_t ro = __builtin_amdgcn_make_buffer_rsrc((void*)out_row, 0, G * D * 2, 0x00020000);
      __builtin_amdgcn_raw_buffer_store_b128(u32x4{0u, 0u, 0u, 0u}, ro, lane * 16, 0, ZAUX);
    }
    if (lane == 0) __hip_atomic_store(my_nonce, nonce, RLX_AGENT);
    asm volatile("s_waitcnt vmcnt(0)" ::: "memory");
#pragma unroll
    for (int i = 0; i < HS_DELAY; ++i) __builtin_amdgcn_s_sleep(127);
    unsigned long long echoed = nonce;
    bool first = true;
#pragma nounroll
    for (unsigned spins = 0; spins < SPIN_CAP; ++spins) {
      const unsigned long long e = __hip_atomic_load(pt_nonce, RLX_AGENT);
      const unsigned long long r = __hip_atomic_load(pt_echo, RLX_AGENT);
      if (__builtin_amdgcn_readfirstlane((int)(first || e != echoed))) { if (lane == 0) __hip_atomic_store(my_echo, e, RLX_AGENT); echoed = e; first = false; }
      if (__builtin_amdgcn_readfirstlane((int)(r == nonce))) break;
      __builtin_amdgcn_s_sleep(16);
    }
    const unsigned long long pt = __hip_atomic_load(pt_nonce, RLX_AGENT);
    if (__builtin_amdgcn_readfirstlane((int)(pt != echoed))) { if (lane == 0) __hip_atomic_store(my_echo, pt, RLX_AGENT); }

    gu64* const my_times = (gu64*)(ws_me + WS_TIME);
    gu64* const mate_times = (gu64*)((GAS char*)ws + (size_t)((cid & ~7) + (lane & 7)) * WS_WG + WS_TIME);
    unsigned long long prev = t_entry + STARTUP_TICKS;
#pragma nounroll
    for (int j = 0; j < NMILE; ++j) {
      unsigned spins = 0;
      while (ctl[C_MILE] < (unsigned)(NWAVE * (j + 1)) && ++spins < SPIN_CAP) __builtin_amdgcn_s_sleep(PACE_POLL);
      const unsigned long long t64 = __builtin_amdgcn_s_memrealtime();
      int back = 0;
#pragma unroll
      for (int ww = 0; ww < NWAVE; ++ww) back += (int)((unsigned)t64 - ctl[C_MT + ww]);
      const unsigned long long t_me = t64 - (unsigned long long)(__builtin_amdgcn_readfirstlane(back) / NWAVE);
      if (lane == 0) __hip_atomic_store(my_times + j, t_me, RLX_AGENT);
      int d = 0;
#pragma nounroll
      for (int tries = 0; tries < PACE_TRIES; ++tries) {
        __builtin_amdgcn_s_sleep(PACE_WAIT);
        const unsigned long long ti = lane < 8 ? __hip_atomic_load(mate_times + j, RLX_AGENT) : t_me;
        const unsigned long long t_now = __builtin_amdgcn_s_memrealtime();
        const bool valid = ti + 500ull >= t_start && ti <= t_now;
        d = lane < 8 ? (int)((valid ? ti : t_now) - t_me) : 0;
        if (__all(valid)) break;
      }
      d += __builtin_amdgcn_mov_dpp(d, 0xB1, 0xf, 0xf, true);
      d += __builtin_amdgcn_mov_dpp(d, 0x4E, 0xf, 0xf, true);
      d += __builtin_amdgcn_mov_dpp(d, 0x141, 0xf, 0xf, true);
      const unsigned long long mean = t_me + (long long)(__builtin_amdgcn_readfirstlane(d) / 8);
      const unsigned p16 = (unsigned)((mean - prev) * 16ull / MPER);
      const unsigned t0 = (unsigned)mean - ((MPER * (j + 1) * p16) >> 4) + SCHED_BIAS;
      prev = mean;
      if (lane == 0) *(LAS volatile unsigned long long*)(ctl + C_SCHED) = ((unsigned long long)p16 << 32) | t0;
      if (MPER * (j + 1) == NOCT) {
        unsigned sp2 = 0;
        while (ctl[C_LDONE] < (unsigned)NWAVE && ++sp2 < SPIN_CAP) __builtin_amdgcn_s_sleep(8);
        const int hh = lane & 3;
        float M = mw[hh];
#pragma unroll
        for (int ww = 1; ww < NWAVE; ++ww) M = fmaxf(M, mw[ww * 4 + hh]);
        M = cut24(M);
        float L = 0.f;
#pragma unroll
        for (int ww = 0; ww < NWAVE; ++ww) L = fmaf(__builtin_amdgcn_exp2f(mw[ww * 4 + hh] - M), lw[ww * 4 + hh], L);
        const __amdgpu_buffer_rsrc_t rout = __builtin_amdgcn_make_buffer_rsrc((void*)(ws_pt + WS_INBOX), 0, 64, 0x00020000);
        const __amdgpu_buffer_rsrc_t rin = __builtin_amdgcn_make_buffer_rsrc((void*)(ws_me + WS_INBOX), 0, 64, 0x00020000);
        if (lane < 4) __builtin_amdgcn_raw_buffer_store_b128(granules(M, L, pt & 0xffffffffffull), rout, lane * 16, 0, AUX_SC1);
        u32x4 g;
#pragma nounroll
        for (unsigned spins2 = 0; spins2 < SPIN_CAP; ++spins2) {
          asm volatile("" ::: "memory");
          g = __builtin_amdgcn_raw_buffer_load_b128(rin, hh * 16, 0, AUX_SC1);
          if (__all(granules_bad(g, nonce & 0xffffffffffull) == 0u)) break;
          __builtin_amdgcn_s_sleep(8);
        }
        const float Lt = cut24(L), Mi = gran_a(g), Li = gran_b(g);
        const float Ma = (cid & 1) ? Mi : M, Mb = (cid & 1) ? M : Mi, La = (cid & 1) ? Li : Lt, Lb = (cid & 1) ? Lt : Li;
        const float Mx = fmaxf(Ma, Mb);
        const float tot = fmaf(__builtin_amdgcn_exp2f(Ma - Mx), La, __builtin_amdgcn_exp2f(Mb - Mx) * Lb);
        if (lane < 4) { nm[lane] = Mx; nm[4 + lane] = 1.0f / tot; }
        asm volatile("s_waitcnt lgkmcnt(0)" ::: "memory");
        if (lane == 0) ctl[C_NORM] = 1u;
      }
    }
    return;
  }
  const __amdgpu_buffer_rsrc_t rk = __builtin_amdgcn_make_buffer_rsrc((void*)(k + (size_t)cid * (KEYS_WG * D)), 0, WG_BYTES, 0x00020000);
  const __amdgpu_buffer_rsrc_t rv = __builtin_amdgcn_make_buffer_rsrc((void*)(v + (size_t)cid * (KEYS_WG * D)), 0, WG_BYTES, 0x00020000);
  const __amdgpu_buffer_rsrc_t rz = __builtin_amdgcn_make_buffer_rsrc((void*)k, 0, 0, 0x00020000);
  const int koff = ((lane >> 2) & 3) * 256 + (lane >> 4) * 64 + (lane & 3) * 16;
  const int voff = lane * 16;
  const int p = lane & 3;
  const int wbase = w * 4096;

  u32x4 qf;
  asm volatile("global_load_dwordx4 %0, %1, %2 nt" : "=v"(qf) : "v"(koff), "s"((const GAS char*)q + (size_t)bh * (G * D * 2)) : "memory");
  u32x4 ring[8];
  unsigned wide = 0, junk = 0;
#pragma unroll
  for (int i = 0; i < WN; ++i) first_load(ring[i], rk, koff, wbase + ld_off(i));
  __builtin_amdgcn_s_barrier();
#pragma unroll
  for (int i = WN; i < 7; ++i) next_load(ring[i], qf, ring[0], 0u, rk, koff, wbase + ld_off(i));
  STAMP(1);
  LAS volatile unsigned long long* const schedp = (LAS volatile unsigned long long*)(ctl + C_SCHED);
  unsigned now = 0; unsigned long long sched = 0;
  auto pace = [&](int o) {
    const unsigned t0 = __builtin_amdgcn_readfirstlane((unsigned)sched), p16 = __builtin_amdgcn_readfirstlane((unsigned)(sched >> 32));
    const unsigned nw = __builtin_amdgcn_readfirstlane(now);
    wide = __builtin_amdgcn_readfirstlane(p16 == 0u ? ((DUTY_PAT >> (o & 7)) & 1u) : ((int)(nw - t0 - ((o * p16) >> 4)) > 0 ? 1u : 0u));
    if (o != 0 && (o & (MPER - 1)) == 0) { if (lane == 0) ctl[C_MT + w] = now; lds_add(ctl + C_MILE, lane); }
  };
  auto fetch_pace = [&]() { now = (unsigned)__builtin_amdgcn_s_memrealtime(); sched = *schedp; };

  f32x2* sc = (f32x2*)lds + w * NOCT * 64 + lane;
  float m = -INFINITY;
#pragma nounroll
  for (int it = 0; it < NOCT; ++it) {
    const bool last = it == NOCT - 1;
    const __amdgpu_buffer_rsrc_t rn = last ? rv : rk;
    const int noff = last ? voff : koff;
    const int cbase = wbase + it * OCT_BYTES;
    const int nbase = last ? wbase : cbase + OCT_BYTES;
    pace(it);
    f32x2 sp;
#pragma unroll
    for (int n = 0; n < 2; ++n) {
      f32x4 d[4];
#pragma unroll
      for (int j = 0; j < 4; ++j) {
        const int i = 4 * n + j, t = i + 7;
        if (t < 8) next_load(ring[t & 7], ring[i], ring[(i + 1) & 7], wide, rk, koff, cbase + ld_off(t));
        else next_load(ring[t & 7], ring[i], ring[(i + 1) & 7], wide, rn, noff, nbase + ld_off(t - 8));
        __builtin_amdgcn_sched_barrier(0);
        d[j] = mfma16(ring[i], qf, f32x4{0.f, 0.f, 0.f, 0.f});
        __builtin_amdgcn_sched_barrier(0);
        if ((NDUMMY >= 1 && i == 1) || (NDUMMY >= 2 && i == 5)) { narrow_pad(junk, wide, rz, koff); __builtin_amdgcn_sched_barrier(0); }
      }
      if (n == 1) { fetch_pace(); __builtin_amdgcn_sched_barrier(0); }
      float s[4];
#pragma unroll
      for (int j = 0; j < 4; ++j) s[j] = dpp<0x00>(d[j][0]);
#pragma unroll
      for (int j = 0; j < 4; ++j) s[j] += dpp<0x55>(d[j][1]);
#pragma unroll
      for (int j = 0; j < 4; ++j) s[j] += dpp<0xAA>(d[j][2]);
#pragma unroll
      for (int j = 0; j < 4; ++j) { s[j] += dpp<0xFF>(d[j][3]); asm("" : "+v"(s[j])); }
      float sv = s[0];
      sv = p == 1 ? s[1] : sv;
      sv = p == 2 ? s[2] : sv;
      sv = p == 3 ? s[3] : sv;
      m = fmaxf(m, sv);
      sp[n] = sv;
      __builtin_amdgcn_sched_barrier(0);
    }
    sc[it * 64] = sp;
  }
  STAMP(3);
  m = fmaxf(m, dpp<0xB1>(m));
  m = fmaxf(m, dpp<0x4E>(m));
  m = xrow_max(m);
  const float mc = m * SCALE_LOG2E;
  if ((lane & ~12) == 0) mw[w * 4 + (lane >> 2)] = mc;
  const unsigned lo_sel = (p & 1) ? 0x01000c0cu : 0x0c0c0100u, hi_sel = (p & 1) ? 0x03020c0cu : 0x0c0c0302u, zsel = 0x0c0c0c0cu;
  const unsigned sel0 = p < 2 ? lo_sel : zsel, sel1 = p < 2 ? zsel : lo_sel, sel2 = p < 2 ? hi_sel : zsel, sel3 = p < 2 ? zsel : hi_sel;
  float l = 0.f;
  f32x4 acc[8];
#pragma unroll
  for (int e = 0; e < 8; ++e) acc[e] = f32x4{0.f, 0.f, 0.f, 0.f};
  f32x2 spn = sc[0];
#pragma nounroll
  for (int it = 0; it < NOCT - 1; ++it) {
    pace(NOCT + it);
    const f32x2 sp = spn;
    v_octet<false>(ring, acc, sp, mc, sel0, sel1, sel2, sel3, rv, rz, voff, wbase + it * OCT_BYTES, wide, junk, [&]() {
      fetch_pace();
      spn = sc[(it + 1) * 64];
      if (it < LPRE) {
#pragma unroll
        for (int u = 0; u < NOCT / LPRE; ++u) {
          const f32x2 x = sc[(it * (NOCT / LPRE) + u) * 64];
          l += __builtin_amdgcn_exp2f(fmaf(x[0], SCALE_LOG2E, -mc)) + __builtin_amdgcn_exp2f(fmaf(x[1], SCALE_LOG2E, -mc));
        }
        if (it == LPRE - 1) {
          float t = l;
          t += dpp<0xB1>(t);
          t += dpp<0x4E>(t);
          t = xrow_sum(t);
          if ((lane & ~12) == 0) lw[w * 4 + (lane >> 2)] = t;
          lds_add(ctl + C_LDONE, lane);
        }
      }
    });
  }
  const int h = lane >> 4;
  { unsigned spins = 0; while (ctl[C_NORM] == 0u && ++spins < SPIN_CAP) __builtin_amdgcn_s_sleep(1); }
  const float fo = __builtin_amdgcn_exp2f(mw[w * 4 + h] - nm[h]) * nm[4 + h];
  v_octet<true>(ring, acc, spn, mc, sel0, sel1, sel2, sel3, rv, rz, voff, wbase + (NOCT - 1) * OCT_BYTES, 1u, junk, []() {});
  asm volatile("" :: "v"(junk));
  STAMP(4);
  {
    float* rec = lds + LDS_SC + w * LDS_REC;
    f32x4 o0, o1;
#pragma unroll
    for (int e = 0; e < 4; ++e) {
      o0[e] = ((acc[e][0] + acc[e][1]) + (acc[e][2] + acc[e][3])) * fo;
      o1[e] = ((acc[4 + e][0] + acc[4 + e][1]) + (acc[4 + e][2] + acc[4 + e][3])) * fo;
    }
    *(f32x4*)(rec + lane * 8) = o0;
    *(f32x4*)(rec + lane * 8 + 4) = o1;
  }
  const unsigned arrived = lds_add_rtn(ctl + C_CNT, lane);
  STAMP(5);
  if (arrived == NWAVE - 1) {
    const float* r0 = lds + LDS_SC;
#pragma unroll
    for (int i = 0; i < 4; ++i) {
      f32x2 o = *(const f32x2*)(r0 + i * D + 2 * lane);
#pragma unroll
      for (int ww = 1; ww < NWAVE; ++ww) o += *(const f32x2*)(r0 + ww * LDS_REC + i * D + 2 * lane);
      const bf16x2 r = {(__bf16)o[0], (__bf16)o[1]};
      __builtin_amdgcn_global_atomic_fadd_v2bf16((GAS bf16x2*)(out_row + i * 256) + lane, r);
    }
  }
  STAMP(6);
  STAMP_FLUSH();
}

}

extern "C" void kernel_launch(void* const* d_in, const int* in_sizes, int n_in, void* d_out, int out_size, void* d_ws,
                              size_t ws_size, hipStream_t stream) {
  using namespace gqa;
  char* ws = (char*)d_ws + ((ws_size - (size_t)NWG * WS_WG) & ~(size_t)4095);
  attn_split<<<NWG, NTHR, 0, stream>>>((const unsigned short*)d_in[0], (const unsigned short*)d_in[1], (const unsigned short*)d_in[2], ws,
                                       (unsigned short*)d_out);
}
```

```cpp
#include <hip/hip_runtime.h>
#include <hip/hip_bf16.h>
#include <stdint.h>

namespace gqa {
#define STAMP(i)
#define STAMP_FLUSH()

constexpr int B = 16, HQ = 32, HKV = 8, SK = 4096, D = 128, G = HQ / HKV;
constexpr int NSPLIT = 2;
constexpr int NWG = B * HKV * NSPLIT;
constexpr int NWAVE = 4;
constexpr int WN = 5;
constexpr int WW = 6;
constexpr int WINIT = 6;
constexpr int MPER = 8;
constexpr int TEAM = 8, TEAM_STEP = 33;
constexpr int STARTUP_TICKS = 90;
constexpr int SCHED_BIAS = 10;
constexpr int NTHR = (NWAVE + 1) * 64;
constexpr int KEYS_WG = SK / NSPLIT;
constexpr int NL = KEYS_WG / NWAVE / 4;
constexpr int NOCT = NL / 8;
constexpr int NMILE = 2 * NOCT / MPER - 1;
constexpr int LPRE = 4;
constexpr unsigned WG_BYTES = KEYS_WG * D * 2;
constexpr float SCALE_LOG2E = 0.08838834764831845f * 1.4426950408889634f;

typedef unsigned u32x4 __attribute__((ext_vector_type(4)));
typedef float f32x4 __attribute__((ext_vector_type(4)));
typedef float f32x2 __attribute__((ext_vector_type(2)));
typedef __bf16 bf16x8 __attribute__((ext_vector_type(8)));
typedef __bf16 bf16x2 __attribute__((ext_vector_type(2)));
#define GAS __attribute__((address_space(1)))
#define LAS __attribute__((address_space(3)))
typedef GAS unsigned long long gu64;
typedef LAS volatile unsigned lds_vu32;
typedef LAS volatile float lds_vf32;
#define RLX_AGENT __ATOMIC_RELAXED, __HIP_MEMORY_SCOPE_AGENT
constexpr int AUX_SC1 = 16, AUX_NT = 2;

__device__ __forceinline__ f32x4 mfma16(u32x4 a, u32x4 b, f32x4 c) {
  return __builtin_amdgcn_mfma_f32_16x16x32_bf16(__builtin_bit_cast(bf16x8, a), __builtin_bit_cast(bf16x8, b), c, 0, 0, 0);
}
template <int CTRL> __device__ __forceinline__ float dpp(float x) {
  return __builtin_bit_cast(float, __builtin_amdgcn_mov_dpp(__builtin_bit_cast(int, x), CTRL, 0xf, 0xf, true));
}
__device__ __forceinline__ float xrow_max(float x) {
  auto s = __builtin_amdgcn_permlane16_swap(__float_as_uint(x), __float_as_uint(x), false, false);
  x = fmaxf(__uint_as_float(s[0]), __uint_as_float(s[1]));
  auto t = __builtin_amdgcn_permlane32_swap(__float_as_uint(x), __float_as_uint(x), false, false);
  return fmaxf(__uint_as_float(t[0]), __uint_as_float(t[1]));
}
__device__ __forceinline__ float xrow_sum(float x) {
  auto s = __builtin_amdgcn_permlane16_swap(__float_as_uint(x), __float_as_uint(x), false, false);
  x = __uint_as_float(s[0]) + __uint_as_float(s[1]);
  auto t = __builtin_amdgcn_permlane32_swap(__float_as_uint(x), __float_as_uint(x), false, false);
  return __uint_as_float(t[0]) + __uint_as_float(t[1]);
}
__device__ __forceinline__ unsigned pack_bf16(float lo, float hi) {
  bf16x2 r = {(__bf16)lo, (__bf16)hi};
  return __builtin_bit_cast(unsigned, r);
}
__device__ __forceinline__ unsigned lds_add_rtn(lds_vu32* p, int lane) {
  unsigned r;
  const unsigned inc = lane == 0 ? 1u : 0u, addr = (unsigned)(size_t)p;
  asm volatile("ds_add_rtn_u32 %0, %1, %2\n\ts_waitcnt lgkmcnt(0)" : "=v"(r) : "v"(addr), "v"(inc) : "memory");
  return __builtin_amdgcn_readfirstlane(r);
}
__device__ __forceinline__ void lds_add(lds_vu32* p, int lane) {
  const unsigned inc = lane == 0 ? 1u : 0u, addr = (unsigned)(size_t)p;
  asm volatile("ds_add_u32 %0, %1" :: "v"(addr), "v"(inc) : "memory");
}
constexpr int ld_off(int j) { return ((j >> 2) * (4 * NWAVE) + (j & 3)) * 1024; }
constexpr int OCT_BYTES = 8 * NWAVE * 1024;

constexpr int WS_WG = 1024, WS_INBOX = 0, WS_NONCE = 256, WS_ECHO = 512, WS_TIME = 768;
constexpr unsigned SPIN_CAP = 1u << 20;
__device__ __forceinline__ u32x4 granules(float a, float b, unsigned long long tag40) {
  const unsigned lo = (unsigned)tag40 << 24, hi = (unsigned)(tag40 >> 8);
  return u32x4{lo | (__float_as_uint(a) >> 8), hi, lo | (__float_as_uint(b) >> 8), hi};
}
__device__ __forceinline__ unsigned granules_bad(u32x4 g, unsigned long long tag40) {
  const unsigned lo = (unsigned)tag40 & 0xffu, hi = (unsigned)(tag40 >> 8);
  return ((g[0] >> 24) ^ lo) | (g[1] ^ hi) | ((g[2] >> 24) ^ lo) | (g[3] ^ hi);
}
__device__ __forceinline__ float cut24(float x) { return __uint_as_float(__float_as_uint(x) & 0xffffff00u); }
__device__ __forceinline__ float gran_a(u32x4 g) { return __uint_as_float(g[0] << 8); }
__device__ __forceinline__ float gran_b(u32x4 g) { return __uint_as_float(g[2] << 8); }

constexpr int LDS_SC = NWAVE * NOCT * 2 * 64;
constexpr int LDS_REC = 4 * D;
constexpr int LDS_MW = LDS_SC + NWAVE * LDS_REC;
constexpr int LDS_LW = LDS_MW + NWAVE * 4;
constexpr int LDS_NM = LDS_LW + NWAVE * 4;
constexpr int LDS_CTL = LDS_NM + 8;
enum { C_CNT = 0, C_LDONE = 1, C_NORM = 2, C_MILE = 3, C_SCHED = 4  , C_MT = 8  , C_WORDS = 16 };

__device__ __forceinline__ void first_load(u32x4& x, __amdgpu_buffer_rsrc_t r, int voff, int soff) {
  asm volatile("buffer_load_dwordx4 %0, %1, %2, %3 offen nt" : "=v"(x) : "v"(voff), "s"(r), "s"(soff) : "memory");
}
__device__ __forceinline__ void next_load(u32x4& x, u32x4& a, u32x4& b, unsigned wide, __amdgpu_buffer_rsrc_t r, int voff, int soff) {
  asm volatile("s_cmp_lg_u32 %3, 0\n\ts_cbranch_scc1 1f\n\t"
               "s_waitcnt vmcnt(%7)\n\tbuffer_load_dwordx4 %0, %4, %5, %6 offen nt\n\ts_branch 2f\n"
               "1:\n\ts_waitcnt vmcnt(%8)\n\tbuffer_load_dwordx4 %0, %4, %5, %6 offen nt\n"
               "2:"
               : "=&v"(x), "+v"(a), "+v"(b) : "s"(wide), "v"(voff), "s"(r), "s"(soff), "n"(WN - 1), "n"(WW - 1) : "scc", "memory");
}
template <int N> __device__ __forceinline__ void wait_loads(u32x4& a, u32x4& b) {
  asm volatile("s_waitcnt vmcnt(%2)" : "+v"(a), "+v"(b) : "n"(N) : "memory");
}
static_assert(WN >= 2 && WN <= WW && WW <= 6 && WINIT >= WN && WINIT <= 6, "a pair's two loads must have landed behind next_load's wait");

template <bool LAST, class Tail>
__device__ __forceinline__ void v_octet(u32x4 (&ring)[8], f32x4 (&acc)[8], const f32x2 sp, const float mc, const unsigned sel0, const unsigned sel1,
                                        const unsigned sel2, const unsigned sel3, const __amdgpu_buffer_rsrc_t rv, const int voff, const int cbase,
                                        const unsigned wide, Tail&& tail) {
  u32x4 blo[4], bhi[4], pa;
#pragma unroll
  for (int mm = 0; mm < 4; ++mm) {
    if (!LAST || mm == 0) {
      next_load(ring[(2 * mm + 7) & 7], ring[2 * mm], ring[2 * mm + 1], wide, rv, voff, cbase + (mm == 0 ? ld_off(7) : OCT_BYTES + ld_off(2 * mm + 7 - 8)));
    }
    else if (mm == 1) wait_loads<4>(ring[2], ring[3]);
    else if (mm == 2) wait_loads<2>(ring[4], ring[5]);
    else wait_loads<0>(ring[6], ring[7]);
    __builtin_amdgcn_sched_barrier(0);
    const u32x4 a = ring[2 * mm], b = ring[2 * mm + 1];
#pragma unroll
    for (int wd = 0; wd < 4; ++wd) {
      blo[wd][mm] = __builtin_amdgcn_perm(b[wd], a[wd], 0x05040100u);
      bhi[wd][mm] = __builtin_amdgcn_perm(b[wd], a[wd], 0x07060302u);
    }
    asm volatile("" : "+v"(blo[0][mm]), "+v"(blo[1][mm]), "+v"(blo[2][mm]), "+v"(blo[3][mm]), "+v"(bhi[0][mm]), "+v"(bhi[1][mm]), "+v"(bhi[2][mm]),
                 "+v"(bhi[3][mm]) :: "memory");
    __builtin_amdgcn_sched_barrier(0);
    if (!LAST) {
      next_load(ring[2 * mm], ring[(2 * mm + 2) & 7], ring[(2 * mm + 3) & 7], wide, rv, voff, cbase + OCT_BYTES + ld_off(2 * mm + 8 - 8));
      __builtin_amdgcn_sched_barrier(0);
    }
    if (mm == 0) {
      const float p0 = __builtin_amdgcn_exp2f(fmaf(sp[0], SCALE_LOG2E, -mc));
      const float p1 = __builtin_amdgcn_exp2f(fmaf(sp[1], SCALE_LOG2E, -mc));
      const unsigned pk = pack_bf16(p0, p1);
      pa = u32x4{__builtin_amdgcn_perm(0u, pk, sel0), __builtin_amdgcn_perm(0u, pk, sel1), __builtin_amdgcn_perm(0u, pk, sel2),
                 __builtin_amdgcn_perm(0u, pk, sel3)};
      asm volatile("" : "+v"(pa));
      __builtin_amdgcn_sched_barrier(0);
    }
  }
  tail();
  __builtin_amdgcn_sched_barrier(0);
#pragma unroll
  for (int wd = 0; wd < 4; ++wd) {
    acc[2 * wd] = mfma16(pa, blo[wd], acc[2 * wd]);
    acc[2 * wd + 1] = mfma16(pa, bhi[wd], acc[2 * wd + 1]);
  }
  __builtin_amdgcn_sched_barrier(0);
}

__global__ void __launch_bounds__(NTHR, 2)
attn_split(const unsigned short* __restrict__ q, const unsigned short* __restrict__ k, const unsigned short* __restrict__ v,
           char* __restrict__ ws, unsigned short* __restrict__ out) {
  __shared__ __attribute__((aligned(16))) float lds[LDS_CTL + C_WORDS];
  STAMP(0);
  asm volatile("" :: "s"(q), "s"(k), "s"(v), "s"(ws), "s"(out));
  const int tid = threadIdx.x, lane = tid & 63;
  const int w = __builtin_amdgcn_readfirstlane(tid >> 6);
  const int cid = blockIdx.x, bh = cid >> 1;
  lds_vu32* const ctl = (lds_vu32*)(LAS float*)lds + LDS_CTL;
  lds_vf32* const mw = (lds_vf32*)(LAS float*)lds + LDS_MW;
  lds_vf32* const lw = (lds_vf32*)(LAS float*)lds + LDS_LW;
  lds_vf32* const nm = (lds_vf32*)(LAS float*)lds + LDS_NM;
  GAS char* const out_row = (GAS char*)out + (size_t)bh * (G * D * 2);
  if (w == NWAVE) {
    const unsigned long long t_entry = __builtin_amdgcn_s_memrealtime();
    if (lane < C_WORDS) ctl[lane] = 0u;
    asm volatile("s_waitcnt lgkmcnt(0)" ::: "memory");
    __builtin_amdgcn_s_barrier();
    GAS char* const ws_me = (GAS char*)ws + (size_t)cid * WS_WG;
    GAS char* const ws_pt = (GAS char*)ws + (size_t)(cid ^ 1) * WS_WG;
    gu64* const my_nonce = (gu64*)(ws_me + WS_NONCE);
    gu64* const my_echo = (gu64*)(ws_me + WS_ECHO);
    gu64* const pt_nonce = (gu64*)(ws_pt + WS_NONCE);
    gu64* const pt_echo = (gu64*)(ws_pt + WS_ECHO);
    __builtin_amdgcn_s_sleep(127);
    const unsigned long long t_start = __builtin_amdgcn_s_memrealtime();
    const unsigned long long nonce = __hip_atomic_load(my_nonce, RLX_AGENT) + 1ull;
    if (!(cid & 1)) {
      const __amdgpu_buffer_rsrc_t ro = __builtin_amdgcn_make_buffer_rsrc((void*)out_row, 0, G * D * 2, 0x00020000);
      __builtin_amdgcn_raw_buffer_store_b128(u32x4{0u, 0u, 0u, 0u}, ro, lane * 16, 0, AUX_SC1 | AUX_NT);
    }
    if (lane == 0) __hip_atomic_store(my_nonce, nonce, RLX_AGENT);
    asm volatile("s_waitcnt vmcnt(0)" ::: "memory");
    __builtin_amdgcn_s_sleep(127);
    unsigned long long echoed = nonce;
    bool first = true;
#pragma nounroll
    for (unsigned spins = 0; spins < SPIN_CAP; ++spins) {
      const unsigned long long e = __hip_atomic_load(pt_nonce, RLX_AGENT);
      const unsigned long long r = __hip_atomic_load(pt_echo, RLX_AGENT);
      if (__builtin_amdgcn_readfirstlane((int)(first || e != echoed))) { if (lane == 0) __hip_atomic_store(my_echo, e, RLX_AGENT); echoed = e; first = false; }
      if (__builtin_amdgcn_readfirstlane((int)(r == nonce))) break;
      __builtin_amdgcn_s_sleep(16);
    }
    const unsigned long long pt = __hip_atomic_load(pt_nonce, RLX_AGENT);
    if (__builtin_amdgcn_readfirstlane((int)(pt != echoed))) { if (lane == 0) __hip_atomic_store(my_echo, pt, RLX_AGENT); }

    gu64* const my_times = (gu64*)(ws_me + WS_TIME);
    gu64* const mate_times = (gu64*)((GAS char*)ws + (size_t)((cid + (lane & (TEAM - 1)) * TEAM_STEP) & (NWG - 1)) * WS_WG + WS_TIME);
    unsigned long long prev = t_entry + STARTUP_TICKS;
#pragma nounroll
    for (int j = 0; j < NMILE; ++j) {
      unsigned spins = 0;
      while (ctl[C_MILE] < (unsigned)(NWAVE * (j + 1)) && ++spins < SPIN_CAP) __builtin_amdgcn_s_sleep(8);
      const unsigned long long t64 = __builtin_amdgcn_s_memrealtime();
      int back = 0;
#pragma unroll
      for (int ww = 0; ww < NWAVE; ++ww) back += (int)((unsigned)t64 - ctl[C_MT + ww]);
      const unsigned long long t_me = t64 - (unsigned long long)(__builtin_amdgcn_readfirstlane(back) / NWAVE);
      if (lane == 0) __hip_atomic_store(my_times + j, t_me, RLX_AGENT);
      int d = 0;
#pragma nounroll
      for (int tries = 0; tries < 4; ++tries) {
        __builtin_amdgcn_s_sleep(14);
        const unsigned long long ti = lane < TEAM ? __hip_atomic_load(mate_times + j, RLX_AGENT) : t_me;
        const unsigned long long t_now = __builtin_amdgcn_s_memrealtime();
        const bool valid = ti + 500ull >= t_start && ti <= t_now;
        d = lane < TEAM ? (int)((valid ? ti : t_now) - t_me) : 0;
        if (__all(valid)) break;
      }
      d += __builtin_amdgcn_mov_dpp(d, 0xB1, 0xf, 0xf, true);
      d += __builtin_amdgcn_mov_dpp(d, 0x4E, 0xf, 0xf, true);
      d += __builtin_amdgcn_mov_dpp(d, 0x141, 0xf, 0xf, true);
      const unsigned long long mean = t_me + (long long)(__builtin_amdgcn_readfirstlane(d) / TEAM);
      const unsigned p16 = (unsigned)((mean - prev) * 16ull / MPER);
      const unsigned t0 = (unsigned)mean - ((MPER * (j + 1) * p16) >> 4) + SCHED_BIAS;
      prev = mean;
      if (lane == 0) *(LAS volatile unsigned long long*)(ctl + C_SCHED) = ((unsigned long long)p16 << 32) | t0;
      if (MPER * (j + 1) == NOCT) {
        unsigned sp2 = 0;
        while (ctl[C_LDONE] < (unsigned)NWAVE && ++sp2 < SPIN_CAP) __builtin_amdgcn_s_sleep(8);
        const int hh = lane & 3;
        float M = mw[hh];
#pragma unroll
        for (int ww = 1; ww < NWAVE; ++ww) M = fmaxf(M, mw[ww * 4 + hh]);
        M = cut24(M);
        float L = 0.f;
#pragma unroll
        for (int ww = 0; ww < NWAVE; ++ww) L = fmaf(__builtin_amdgcn_exp2f(mw[ww * 4 + hh] - M), lw[ww * 4 + hh], L);
        const __amdgpu_buffer_rsrc_t rout = __builtin_amdgcn_make_buffer_rsrc((void*)(ws_pt + WS_INBOX), 0, 64, 0x00020000);
        const __amdgpu_buffer_rsrc_t rin = __builtin_amdgcn_make_buffer_rsrc((void*)(ws_me + WS_INBOX), 0, 64, 0x00020000);
        if (lane < 4) __builtin_amdgcn_raw_buffer_store_b128(granules(M, L, pt & 0xffffffffffull), rout, lane * 16, 0, AUX_SC1);
        u32x4 g;
#pragma nounroll
        for (unsigned spins2 = 0; spins2 < SPIN_CAP; ++spins2) {
          asm volatile("" ::: "memory");
          g = __builtin_amdgcn_raw_buffer_load_b128(rin, hh * 16, 0, AUX_SC1);
          if (__all(granules_bad(g, nonce & 0xffffffffffull) == 0u)) break;
          __builtin_amdgcn_s_sleep(8);
        }
        const float Lt = cut24(L), Mi = gran_a(g), Li = gran_b(g);
        const float Ma = (cid & 1) ? Mi : M, Mb = (cid & 1) ? M : Mi, La = (cid & 1) ? Li : Lt, Lb = (cid & 1) ? Lt : Li;
        const float Mx = fmaxf(Ma, Mb);
        const float tot = fmaf(__builtin_amdgcn_exp2f(Ma - Mx), La, __builtin_amdgcn_exp2f(Mb - Mx) * Lb);
        if (lane < 4) { nm[lane] = Mx; nm[4 + lane] = 1.0f / tot; }
        asm volatile("s_waitcnt lgkmcnt(0)" ::: "memory");
        if (lane == 0) ctl[C_NORM] = 1u;
      }
    }
    return;
  }
  const __amdgpu_buffer_rsrc_t rk = __builtin_amdgcn_make_buffer_rsrc((void*)(k + (size_t)cid * (KEYS_WG * D)), 0, WG_BYTES, 0x00020000);
  const __amdgpu_buffer_rsrc_t rv = __builtin_amdgcn_make_buffer_rsrc((void*)(v + (size_t)cid * (KEYS_WG * D)), 0, WG_BYTES, 0x00020000);
  const int koff = ((lane >> 2) & 3) * 256 + (lane >> 4) * 64 + (lane & 3) * 16;
  const int voff = lane * 16;
  const int p = lane & 3;
  const int wbase = w * 4096;

  u32x4 qf;
  asm volatile("global_load_dwordx4 %0, %1, %2 nt" : "=v"(qf) : "v"(koff), "s"((const GAS char*)q + (size_t)bh * (G * D * 2)) : "memory");
  u32x4 ring[8];
  unsigned wide = 0;
#pragma unroll
  for (int i = 0; i < WINIT; ++i) first_load(ring[i], rk, koff, wbase + ld_off(i));
  __builtin_amdgcn_s_barrier();
#pragma unroll
  for (int i = WINIT; i < 7; ++i) next_load(ring[i], qf, ring[0], 0u, rk, koff, wbase + ld_off(i));
  STAMP(1);
  LAS volatile unsigned long long* const schedp = (LAS volatile unsigned long long*)(ctl + C_SCHED);
  unsigned now = 0; unsigned long long sched = 0;
  auto pace = [&](int o) {
    const unsigned t0 = __builtin_amdgcn_readfirstlane((unsigned)sched), p16 = __builtin_amdgcn_readfirstlane((unsigned)(sched >> 32));
    const unsigned nw = __builtin_amdgcn_readfirstlane(now);
    wide = __builtin_amdgcn_readfirstlane((p16 != 0u && (int)(nw - t0 - ((o * p16) >> 4)) > 0) ? 1u : 0u);
    if (o != 0 && (o & (MPER - 1)) == 0) { if (lane == 0) ctl[C_MT + w] = now; lds_add(ctl + C_MILE, lane); }
  };
  auto fetch_pace = [&]() { now = (unsigned)__builtin_amdgcn_s_memrealtime(); sched = *schedp; };

  f32x2* sc = (f32x2*)lds + w * NOCT * 64 + lane;
  float m = -INFINITY;
#pragma nounroll
  for (int it = 0; it < NOCT; ++it) {
    const bool last = it == NOCT - 1;
    const __amdgpu_buffer_rsrc_t rn = last ? rv : rk;
    const int noff = last ? voff : koff;
    const int cbase = wbase + it * OCT_BYTES;
    const int nbase = last ? wbase : cbase + OCT_BYTES;
    pace(it);
    f32x2 sp;
#pragma unroll
    for (int n = 0; n < 2; ++n) {
      f32x4 d[4];
#pragma unroll
      for (int j = 0; j < 4; ++j) {
        const int i = 4 * n + j, t = i + 7;
        if (t < 8) next_load(ring[t & 7], ring[i], ring[(i + 1) & 7], wide, rk, koff, cbase + ld_off(t));
        else next_load(ring[t & 7], ring[i], ring[(i + 1) & 7], wide, rn, noff, nbase + ld_off(t - 8));
        __builtin_amdgcn_sched_barrier(0);
        d[j] = mfma16(ring[i], qf, f32x4{0.f, 0.f, 0.f, 0.f});
        __builtin_amdgcn_sched_barrier(0);
      }
      if (n == 1) { fetch_pace(); __builtin_amdgcn_sched_barrier(0); }
      float s[4];
#pragma unroll
      for (int j = 0; j < 4; ++j) s[j] = dpp<0x00>(d[j][0]);
#pragma unroll
      for (int j = 0; j < 4; ++j) s[j] += dpp<0x55>(d[j][1]);
#pragma unroll
      for (int j = 0; j < 4; ++j) s[j] += dpp<0xAA>(d[j][2]);
#pragma unroll
      for (int j = 0; j < 4; ++j) { s[j] += dpp<0xFF>(d[j][3]); asm("" : "+v"(s[j])); }
      float sv = s[0];
      sv = p == 1 ? s[1] : sv;
      sv = p == 2 ? s[2] : sv;
      sv = p == 3 ? s[3] : sv;
      m = fmaxf(m, sv);
      sp[n] = sv;
      __builtin_amdgcn_sched_barrier(0);
    }
    sc[it * 64] = sp;
  }
  STAMP(3);
  m = fmaxf(m, dpp<0xB1>(m));
  m = fmaxf(m, dpp<0x4E>(m));
  m = xrow_max(m);
  const float mc = m * SCALE_LOG2E;
  if ((lane & ~12) == 0) mw[w * 4 + (lane >> 2)] = mc;
  const unsigned lo_sel = (p & 1) ? 0x01000c0cu : 0x0c0c0100u, hi_sel = (p & 1) ? 0x03020c0cu : 0x0c0c0302u, zsel = 0x0c0c0c0cu;
  const unsigned sel0 = p < 2 ? lo_sel : zsel, sel1 = p < 2 ? zsel : lo_sel, sel2 = p < 2 ? hi_sel : zsel, sel3 = p < 2 ? zsel : hi_sel;
  float l = 0.f;
  f32x4 acc[8];
#pragma unroll
  for (int e = 0; e < 8; ++e) acc[e] = f32x4{0.f, 0.f, 0.f, 0.f};
  f32x2 spn = sc[0];
#pragma nounroll
  for (int it = 0; it < NOCT - 1; ++it) {
    pace(NOCT + it);
    const f32x2 sp = spn;
    v_octet<false>(ring, acc, sp, mc, sel0, sel1, sel2, sel3, rv, voff, wbase + it * OCT_BYTES, wide, [&]() {
      fetch_pace();
      spn = sc[(it + 1) * 64];
      if (it < LPRE) {
#pragma unroll
        for (int u = 0; u < NOCT / LPRE; ++u) {
          const f32x2 x = sc[(it * (NOCT / LPRE) + u) * 64];
          l += __builtin_amdgcn_exp2f(fmaf(x[0], SCALE_LOG2E, -mc)) + __builtin_amdgcn_exp2f(fmaf(x[1], SCALE_LOG2E, -mc));
        }
        if (it == LPRE - 1) {
          float t = l;
          t += dpp<0xB1>(t);
          t += dpp<0x4E>(t);
          t = xrow_sum(t);
          if ((lane & ~12) == 0) lw[w * 4 + (lane >> 2)] = t;
          lds_add(ctl + C_LDONE, lane);
        }
      }
    });
  }
  const int h = lane >> 4;
  { unsigned spins = 0; while (ctl[C_NORM] == 0u && ++spins < SPIN_CAP) __builtin_amdgcn_s_sleep(1); }
  const float fo = __builtin_amdgcn_exp2f(mw[w * 4 + h] - nm[h]) * nm[4 + h];
  v_octet<true>(ring, acc, spn, mc, sel0, sel1, sel2, sel3, rv, voff, wbase + (NOCT - 1) * OCT_BYTES, 1u, []() {});
  STAMP(4);
  {
    float* rec = lds + LDS_SC + w * LDS_REC;
    f32x4 o0, o1;
#pragma unroll
    for (int e = 0; e < 4; ++e) {
      o0[e] = ((acc[e][0] + acc[e][1]) + (acc[e][2] + acc[e][3])) * fo;
      o1[e] = ((acc[4 + e][0] + acc[4 + e][1]) + (acc[4 + e][2] + acc[4 + e][3])) * fo;
    }
    *(f32x4*)(rec + lane * 8) = o0;
    *(f32x4*)(rec + lane * 8 + 4) = o1;
  }
  const unsigned arrived = lds_add_rtn(ctl + C_CNT, lane);
  STAMP(5);
  if (arrived == NWAVE - 1) {
    const float* r0 = lds + LDS_SC;
#pragma unroll
    for (int i = 0; i < 4; ++i) {
      f32x2 o = *(const f32x2*)(r0 + i * D + 2 * lane);
#pragma unroll
      for (int ww = 1; ww < NWAVE; ++ww) o += *(const f32x2*)(r0 + ww * LDS_REC + i * D + 2 * lane);
      const bf16x2 r = {(__bf16)o[0], (__bf16)o[1]};
      __builtin_amdgcn_global_atomic_fadd_v2bf16((GAS bf16x2*)(out_row + i * 256) + lane, r);
    }
  }
  STAMP(6);
  STAMP_FLUSH();
}

}

extern "C" void kernel_launch(void* const* d_in, const int* in_sizes, int n_in, void* d_out, int out_size, void* d_ws,
                              size_t ws_size, hipStream_t stream) {
  using namespace gqa;
  char* ws = (char*)d_ws + ((ws_size - (size_t)NWG * WS_WG) & ~(size_t)4095);
  attn_split<<<NWG, NTHR, 0, stream>>>((const unsigned short*)d_in[0], (const unsigned short*)d_in[1], (const unsigned short*)d_in[2], ws,
                                       (unsigned short*)d_out);
}
```
